```python
import torch

def _softmax(x, axis=-1):
    return torch.softmax(x, dim=axis)
def _gelu(x):
    return torch.nn.functional.gelu(x, approximate="tanh")
def _silu(x):
    return torch.nn.functional.silu(x)
def _sigmoid(x):
    return torch.sigmoid(x)
def _layernorm(x, axis=-1, eps=1e-5):
    m = x.mean(dim=axis, keepdim=True)
    v = x.var(dim=axis, keepdim=True, unbiased=False)
    return (x - m) / torch.sqrt(v + eps)
def _scatter_add(tgt, idx, src, axis=0):
    return tgt.clone().index_add_(axis, idx, src)
def _segment_sum(src, seg, n):
    out = torch.zeros((n,) + src.shape[1:], dtype=src.dtype)
    return out.index_add_(0, seg, src)
def _pad(x, pw):
    flat = tuple(v for pair in reversed(pw) for v in pair)
    return torch.nn.functional.pad(x, flat)

kernel_name = 'concat_heads__ssm_conv_s4d__mha_block__prompt'

def setup_inputs():

    _w = (lambda a: (lambda t: t.to(torch.bfloat16) if t.is_floating_point() else t)(torch.as_tensor(a)))
    def _A_setup():
        Bsz, L, D, N = 1, 2048, 640, 128
        return {
            "u": _w((lambda *s: torch.randn(*s, dtype=torch.bfloat16) * 0.05)(Bsz, L, D)),
            "log_A_real": _w((lambda *s: torch.randn(*s, dtype=torch.bfloat16) * 3.2)(D, N) * 0.5),
            "A_imag": _w((lambda *s: torch.randn(*s, dtype=torch.bfloat16) * 0.1)(D, N) * 3.14),
            "C_real": _w((lambda *s: torch.randn(*s, dtype=torch.bfloat16) * 1.6)(D, N) / N ** 0.5),
            "C_imag": _w((lambda *s: torch.randn(*s, dtype=torch.bfloat16) * 1.6)(D, N) / N ** 0.5),
            "log_dt": _w((lambda *s: torch.randn(*s, dtype=torch.bfloat16) * 0.1)(D) * 0.5 - 4.0),
            "D_skip": _w((lambda *s: torch.randn(*s, dtype=torch.bfloat16) * 0.1)(D)),
        }
    _A_in = _A_setup()

    def _B_setup():
        B, S, D, H = 1, 2048, 640, 32
        return {
            "x": _w((lambda *s: torch.randn(*s, dtype=torch.bfloat16) * 0.2)(B, S, D)),
            "wqkv": _w((lambda *s: torch.randn(*s, dtype=torch.bfloat16) * 0.2)(D, 3 * D)),
            "wo": _w((lambda *s: torch.randn(*s, dtype=torch.bfloat16) * 0.05)(D, D)),
        }
    _B_in = _B_setup()

    return {
        "A_u": _A_in["u"],
        "A_log_A_real": _A_in["log_A_real"],
        "A_A_imag": _A_in["A_imag"],
        "A_C_real": _A_in["C_real"],
        "A_C_imag": _A_in["C_imag"],
        "A_log_dt": _A_in["log_dt"],
        "A_D_skip": _A_in["D_skip"],
        "B_x": _B_in["x"],
        "B_wqkv": _B_in["wqkv"],
        "B_wo": _B_in["wo"],
    }

def reference(A_u, A_log_A_real, A_A_imag, A_C_real, A_C_imag, A_log_dt, A_D_skip, B_x, B_wqkv, B_wo):
    def _A_ref(u, log_A_real, A_imag, C_real, C_imag, log_dt, D_skip):
        L = u.shape[1]
        dt = torch.exp(log_dt)
        A = -torch.exp(log_A_real) + 1j * A_imag
        C = (C_real + 1j * C_imag) * dt[:, None]
        pos = (lambda x, dt: x.to(dt))(torch.arange(L), log_dt.dtype)
        P = torch.exp((A * dt[:, None])[:, :, None] * pos[None, None, :])
        K = 2.0 * torch.einsum("dn,dnl->dl", C, P).real
        L2 = 2 * L
        u_t = (lambda x, *d: x.permute(*d))(u, 0, 2, 1)
        Kf = (lambda x, *a, **k: torch.fft.rfft(x.float(), *a, **k))(K, L2)
        Uf = (lambda x, *a, **k: torch.fft.rfft(x.float(), *a, **k))(u_t, L2)
        y = (lambda x, *a, **k: torch.fft.irfft(x, *a, **k).to(torch.bfloat16))(Uf * Kf[None, :, :], L2)[:, :, :L]
        y = (lambda x, *d: x.permute(*d))(y, 0, 2, 1)
        return y + u * D_skip[None, None, :]

    def _B_ref(x, wqkv, wo):
        B, S, D = x.shape
        H = 32
        Dh = D // H
        qkv = (lambda x, *d: x.permute(*d))((x @ wqkv).reshape(B, S, 3, H, Dh), 2, 0, 3, 1, 4)
        q, k, v = qkv[0], qkv[1], qkv[2]
        scores = torch.einsum("bhqd,bhkd->bhqk", q, k) * (Dh ** -0.5)
        p = _softmax(scores, axis=-1)
        o = torch.einsum("bhqk,bhkd->bhqd", p, v)
        return (lambda x, *d: x.permute(*d))(o, 0, 2, 1, 3).reshape(B, S, D) @ wo

    a = _A_ref(A_u, A_log_A_real, A_A_imag, A_C_real, A_C_imag, A_log_dt, A_D_skip)
    b = _B_ref(B_x, B_wqkv, B_wo)
    return torch.concatenate([a, b], axis=-1)
```

```cpp
#include <hip/hip_runtime.h>
#include <hip/hip_bf16.h>
#include <stdint.h>

typedef __bf16 bf16x8 __attribute__((ext_vector_type(8)));
typedef __bf16 bf16x16 __attribute__((ext_vector_type(16)));
typedef __bf16 bf16x2 __attribute__((ext_vector_type(2)));
typedef _Float16 f16x8 __attribute__((ext_vector_type(8)));
typedef _Float16 f16x2 __attribute__((ext_vector_type(2)));
typedef short s16x4 __attribute__((ext_vector_type(4)));
typedef float f32x2 __attribute__((ext_vector_type(2)));
typedef float f32x8 __attribute__((ext_vector_type(8)));
typedef float f32x16 __attribute__((ext_vector_type(16)));
typedef unsigned u32x2 __attribute__((ext_vector_type(2)));
typedef unsigned u32x4 __attribute__((ext_vector_type(4)));
#define LAS __attribute__((address_space(3)))
typedef LAS char* lptr;

constexpr int SEQ = 2048, DM = 640, NST = 128, NH = 32, OUTW = 1280;
constexpr unsigned KV_HEAD = 163840, KV_PAIR = 5120, Q_TILE = 1280, Q_HEAD = 64 * Q_TILE;
constexpr size_t WS_KV = 0, WS_Q = WS_KV + (size_t)NH * KV_HEAD, WS_O = WS_Q + (size_t)NH * Q_HEAD;

__device__ __forceinline__ unsigned pk_bf16(float lo, float hi) {
  bf16x2 v = {(__bf16)lo, (__bf16)hi};
  return __builtin_bit_cast(unsigned, v);
}
__device__ __forceinline__ float bf_lo(unsigned w) { return __uint_as_float(w << 16); }
__device__ __forceinline__ float bf_hi(unsigned w) { return __uint_as_float(w & 0xffff0000u); }
__device__ __forceinline__ float ld_bf(const uint16_t* p) { return __uint_as_float((unsigned)*p << 16); }
__device__ __forceinline__ float rnd_bf(float x) { return (float)(__bf16)x; }
__device__ __forceinline__ int swap23(int r) { return (r & ~12) | ((r & 4) << 1) | ((r & 8) >> 1); }

__device__ __forceinline__ void glds16(const void* sbase, unsigned voff, unsigned lds_dst) {
  unsigned keep;
  asm volatile("s_mov_b32 %0, m0\n\ts_mov_b32 m0, %3\n\ts_nop 0\n\tglobal_load_lds_dwordx4 %1, %2\n\ts_mov_b32 m0, %0"
               : "=&s"(keep) : "v"(voff), "s"(sbase), "s"(lds_dst) : "memory");
}
#define WAIT_VM(n) asm volatile("s_waitcnt vmcnt(" #n ")" ::: "memory")
#define WAIT_VM_BAR(n) asm volatile("s_waitcnt vmcnt(" #n ") lgkmcnt(0)\n\ts_barrier" ::: "memory")
__device__ __forceinline__ s16x4 tr_read(lptr p) {
  return __builtin_amdgcn_ds_read_tr16_b64_v4i16((LAS s16x4*)p);
}
__device__ __forceinline__ bf16x8 cat8(s16x4 a, s16x4 b) {
  typedef short s16x8 __attribute__((ext_vector_type(8)));
  s16x8 v = {a[0], a[1], a[2], a[3], b[0], b[1], b[2], b[3]};
  return __builtin_bit_cast(bf16x8, v);
}
#define MFMA32(a, b, c) __builtin_amdgcn_mfma_f32_32x32x16_bf16(a, b, c, 0, 0, 0)

constexpr int KC_COPY = 4160, KC_SZ = 4 * KC_COPY, U_BLK = 80, U_SZ = 96 * U_BLK, TROW = 272;
__device__ __forceinline__ f32x2 cmul(f32x2 a, f32x2 b) {
  f32x2 r;
  r[0] = a[0] * b[0] - a[1] * b[1];
  r[1] = a[0] * b[1] + a[1] * b[0];
  return r;
}
__device__ __forceinline__ f32x2 cexp_p(float al2, float rev, float p) {
  const float m = __builtin_amdgcn_exp2f(al2 * p);
  const float f = __builtin_amdgcn_fractf(rev * p);
  f32x2 r = {m * __builtin_amdgcn_cosf(f), m * __builtin_amdgcn_sinf(f)};
  return r;
}
__device__ __forceinline__ void tab_put(lptr t, int row, f32x2 v) {
  f16x2 h = {(_Float16)v[0], (_Float16)v[1]};
  *(LAS unsigned*)(t + row * TROW) = __builtin_bit_cast(unsigned, h);
}
__device__ __forceinline__ void put_run(lptr kc, int p, float v0, float v1, float v2, float v3, float v4, float v5,
                                        float v6, bool c0, bool c123) {
  const unsigned d32 = pk_bf16(v3, v2), d10 = pk_bf16(v1, v0), d65 = pk_bf16(v6, v5), d43 = pk_bf16(v4, v3),
                 d54 = pk_bf16(v5, v4), d21 = pk_bf16(v2, v1);
  if (c0) *(LAS u32x2*)(kc + 2 * (2044 - p)) = (u32x2){d32, d10};
  if (c123) {
    lptr q = kc + 2 * (2040 - p);
    *(LAS u32x2*)(q + KC_COPY) = (u32x2){d65, d43};
    *(LAS u32x2*)(q + 2 * KC_COPY) = (u32x2){d54, d32};
    *(LAS u32x2*)(q + 3 * KC_COPY) = (u32x2){d43, d21};
  }
}

__global__ void __launch_bounds__(256) ssm_kernel(const uint16_t* __restrict__ u, const uint16_t* __restrict__ logA,
                                                  const uint16_t* __restrict__ Aim, const uint16_t* __restrict__ Cre,
                                                  const uint16_t* __restrict__ Cim, const uint16_t* __restrict__ logdt,
                                                  const uint16_t* __restrict__ Dsk, uint16_t* __restrict__ out) {
  __shared__ __attribute__((aligned(16))) char smem[4 * KC_SZ + 4 * U_SZ];
  const int tid = threadIdx.x, lane = tid & 63, w = tid >> 6, r = lane & 31, h = lane >> 5;
  const int d0 = ((blockIdx.x & 7) * 20 + (blockIdx.x >> 3)) * 4, d = d0 + w;
  lptr lds = (lptr)smem;
  lptr kc = lds + w * KC_SZ;
  lptr ub = lds + 4 * KC_SZ;

  u32x2 ur[8];
#pragma unroll
  for (int i = 0; i < 8; ++i) ur[i] = *(const u32x2*)(u + (size_t)(8 * tid + i) * DM + d0);

  const float dt = rnd_bf(expf(ld_bf(logdt + d)));
  float la[2], ai[2], cr[2], ci[2];
#pragma unroll
  for (int p = 0; p < 2; ++p) {
    const int o = d * NST + p * 64 + lane;
    la[p] = ld_bf(logA + o); ai[p] = ld_bf(Aim + o); cr[p] = ld_bf(Cre + o); ci[p] = ld_bf(Cim + o);
  }
  float mx = fmaxf(fmaxf(fabsf(cr[0]), fabsf(ci[0])), fmaxf(fabsf(cr[1]), fabsf(ci[1])));
#pragma unroll
  for (int o = 32; o; o >>= 1) mx = fmaxf(mx, __shfl_xor(mx, o));
  const int ex = __builtin_amdgcn_frexp_expf(mx);
  const float kmul = ldexpf(2.0f * dt, ex);

  const int hq = (r >> 2) & 1, iq = (r & 3) + 4 * (r >> 3);
  const int trow = iq < 7 ? iq + 4 * hq : iq < 11 ? iq + 4 + 2 * hq : iq < 13 ? iq + 6 + 2 * hq : iq == 13 ? 21 + hq : iq == 14 ? 23 + hq : 25;
  lptr ea = kc + trow * TROW + 16 * h;
  lptr fa = kc + (26 + r) * TROW + 16 * h;
  lptr tw = kc + 4 * lane;
  f32x16 ka = {0};
#pragma unroll
  for (int p = 0; p < 2; ++p) {
    const float are = -rnd_bf(expf(la[p])) * dt, aim = ai[p] * dt;
    const float al2 = are * 1.4426950408889634f, rev = aim * 0.15915494309189535f;
    const f32x2 w1 = cexp_p(al2, rev, 1.0f), W = cexp_p(al2, rev, 256.0f);
    f32x2 w8 = cexp_p(al2, rev, 8.0f);
    w8[1] = -w8[1];
    const f32x2 w2 = cmul(w1, w1), w4 = cmul(w2, w2);
    const f32x2 c = {ldexpf(cr[p], -ex), ldexpf(ci[p], -ex)};
    f32x2 e = c;
    tab_put(tw, 0, e);
#pragma unroll
    for (int k = 1; k <= 10; ++k) { e = cmul(e, w1); tab_put(tw, k, e); }
    f32x2 g = cmul(c, W);
    e = g;
    tab_put(tw, 11, e);
#pragma unroll
    for (int k = 12; k <= 16; ++k) { e = cmul(e, w2); tab_put(tw, k, e); }
    g = cmul(g, W); tab_put(tw, 17, g); tab_put(tw, 18, cmul(g, w4));
    g = cmul(g, W); tab_put(tw, 19, g); tab_put(tw, 20, cmul(g, w4));
#pragma unroll
    for (int k = 21; k <= 25; ++k) { g = cmul(g, W); tab_put(tw, k, g); }
    f32x2 f = {1.0f, 0.0f};
    tab_put(tw, 26, f);
    f = w8;
    tab_put(tw, 27, f);
#pragma unroll
    for (int k = 2; k < 32; ++k) { f = cmul(f, w8); tab_put(tw, 26 + k, f); }
#pragma unroll
    for (int s = 0; s < 8; ++s) {
      const f16x8 A = *(LAS f16x8*)(ea + 32 * s), B = *(LAS f16x8*)(fa + 32 * s);
      ka = __builtin_amdgcn_mfma_f32_32x32x16_f16(A, B, ka, 0, 0, 0);
    }
  }
  {
    float k[16];
#pragma unroll
    for (int i = 0; i < 16; ++i) k[i] = ka[i] * kmul;
    const bool last = lane == 63;
    float n11 = __shfl_down(k[11], 1), n13 = __shfl_down(k[13], 1), n14 = __shfl_down(k[14], 1);
    const float z13 = __shfl(k[13], 0), z14 = __shfl(k[14], 0), z15 = __shfl(k[15], 0);
    if (last) { n11 = z13; n13 = z14; n14 = z15; }
    put_run(kc, 8 * r + 4 * h, k[0], k[1], k[2], k[3], k[4], last ? k[4] : k[5], k[6], true, true);
    put_run(kc, 256 + 8 * r + 4 * h, k[7], k[7], k[8], k[9], k[9], k[9], last ? k[9] : k[10], true, true);
    put_run(kc, 512 + 8 * lane, k[11], k[11], k[11], k[12], k[12], k[12], n11, true, true);
    put_run(kc, 516 + 8 * lane, k[12], k[12], n11, n11, n11, n11, n11, true, true);
    const float t13 = (r & 1) ? n13 : k[13], t14 = (r & 1) ? n14 : k[14];
    put_run(kc, 1024 + 8 * lane, k[13], k[13], k[13], k[13], t13, n13, n13, true, true);
    put_run(kc, 1028 + 8 * lane, t13, n13, n13, n13, n13, n13, n13, true, true);
    put_run(kc, 1536 + 8 * lane, k[14], k[14], k[14], k[14], t14, n14, n14, true, true);
    put_run(kc, 1540 + 8 * lane, t14, n14, n14, n14, n14, n14, n14, true, !last);
    put_run(kc, -4, 0.f, 0.f, 0.f, 0.f, k[0], k[1], k[2], false, lane == 0);
    if (lane < 32) *(LAS u32x2*)(kc + (lane >> 3) * KC_COPY + 2 * (2048 + 4 * (lane & 7))) = (u32x2){0u, 0u};
  }
  {
    lptr up = ub + 32 * U_BLK + (tid >> 2) * U_BLK + (tid & 3) * 16;
#pragma unroll
    for (int c = 0; c < 4; ++c) {
      u32x4 v;
#pragma unroll
      for (int i = 0; i < 4; ++i)
        v[i] = __builtin_amdgcn_perm(ur[2 * i + 1][c >> 1], ur[2 * i][c >> 1], (c & 1) ? 0x07060302u : 0x05040100u);
      *(LAS u32x4*)(up + c * U_SZ) = v;
    }
    for (int i = tid; i < 4 * 160; i += 256) *(LAS u32x4*)(ub + (i / 160) * U_SZ + (i % 160) * 16) = (u32x4){0u, 0u, 0u, 0u};
  }
  __syncthreads();
  f32x16 y0 = {0}, y1 = {0};
  {
    lptr ka0 = kc + ((3 - r) & 3) * KC_COPY + 2 * (2044 + 8 * h - 4 * (r >> 2));
    lptr ua0 = ub + w * U_SZ + (32 + r) * U_BLK + 16 * h;
#pragma unroll 4
    for (int m = 0; m < 32; ++m) {
#pragma unroll
      for (int ks = 0; ks < 2; ++ks) {
        lptr pl = ka0 - 32 * (2 * m - ks), ph = pl - 32 * 64;
        const bf16x8 Alo = cat8(*(LAS s16x4*)pl, *(LAS s16x4*)(pl + 8));
        const bf16x8 Ahi = cat8(*(LAS s16x4*)ph, *(LAS s16x4*)(ph + 8));
        lptr pu = ua0 - m * U_BLK + 32 * ks;
        const bf16x8 B0 = *(LAS bf16x8*)pu, B1 = *(LAS bf16x8*)(pu + 32 * U_BLK);
        y0 = MFMA32(Alo, B0, y0);
        y1 = MFMA32(Ahi, B0, y1);
        y1 = MFMA32(Alo, B1, y1);
      }
    }
  }
#pragma unroll
  for (int g = 0; g < 4; ++g) {
    lptr yp = kc + r * U_BLK + 16 * g + 8 * h;
    *(LAS u32x2*)yp = (u32x2){pk_bf16(y0[4 * g], y0[4 * g + 1]), pk_bf16(y0[4 * g + 2], y0[4 * g + 3])};
    *(LAS u32x2*)(yp + 32 * U_BLK) = (u32x2){pk_bf16(y1[4 * g], y1[4 * g + 1]), pk_bf16(y1[4 * g + 2], y1[4 * g + 3])};
  }
  __syncthreads();
  {
    u32x4 yv[4];
    float dk[4];
#pragma unroll
    for (int c = 0; c < 4; ++c) {
      yv[c] = *(LAS u32x4*)(lds + c * KC_SZ + (tid >> 2) * U_BLK + (tid & 3) * 16);
      dk[c] = ld_bf(Dsk + d0 + c);
    }
#pragma unroll
    for (int i = 0; i < 8; ++i) {
      float o[4];
#pragma unroll
      for (int c = 0; c < 4; ++c) {
        const float yy = (i & 1) ? bf_hi(yv[c][i >> 1]) : bf_lo(yv[c][i >> 1]);
        const float uu = (c & 1) ? bf_hi(ur[i][c >> 1]) : bf_lo(ur[i][c >> 1]);
        o[c] = yy + rnd_bf(uu * dk[c]);
      }
      *(u32x2*)(out + (size_t)(8 * tid + i) * OUTW + d0) = (u32x2){pk_bf16(o[0], o[1]), pk_bf16(o[2], o[3])};
    }
  }
}

constexpr int G1_STAGE = 32768, G1_NS = 4;
constexpr float QSCALE = 0.22360679774997896f * 1.4426950408889634f;

template <bool VT>
__device__ __forceinline__ void qkv_tile(const uint16_t* __restrict__ x, const uint16_t* __restrict__ wq, char* ws, lptr lds,
                                         int mt, int ng) {
  const int tid = threadIdx.x, lane = tid & 63, r = lane & 31, h = lane >> 5;
  const int w = __builtin_amdgcn_readfirstlane(tid >> 6), wm = w >> 1, wn = w & 1;
  unsigned xoff[4], woff[4];
#pragma unroll
  for (int i = 0; i < 4; ++i) {
    const int row = (w * 4 + i) * 8 + (lane >> 3);
    xoff[i] = (unsigned)((mt * 128 + row) * DM * 2 + (((lane & 7) ^ ((row >> 1) & 7)) << 4));
    const int kr = (w * 4 + i) * 4 + (lane >> 4);
    woff[i] = (unsigned)(kr * 3 * DM * 2 + ng * 256 + (((lane & 15) ^ ((kr & 3) << 2)) << 4));
  }
  const unsigned lbase = (unsigned)(size_t)lds;
  auto stage = [&](int kt) {
    const unsigned dst = lbase + (kt % G1_NS) * G1_STAGE + w * 4096;
    const char* xs = (const char*)x + kt * 128;
    const char* wsrc = (const char*)wq + (size_t)kt * 64 * 3 * DM * 2;
#pragma unroll
    for (int i = 0; i < 4; ++i) glds16(xs, xoff[i], dst + i * 1024);
#pragma unroll
    for (int i = 0; i < 4; ++i) glds16(wsrc, woff[i], dst + 16384 + i * 1024);
  };
  stage(0); stage(1); stage(2);
  const int xr = VT ? swap23(r) : r;
  int xa[2];
#pragma unroll
  for (int t = 0; t < 2; ++t) xa[t] = (wm * 64 + t * 32 + xr) * 128;
  const int xsw = (xr >> 1) & 7;
  const int q = (lane & 15) >> 2, p = lane & 3, blk = (lane >> 4) & 1;
  int wa[2];
#pragma unroll
  for (int t = 0; t < 2; ++t) wa[t] = 16384 + (8 * h + q) * 256 + (((wn * 2 + t) ^ q) << 6) + blk * 32 + p * 8;
  f32x16 acc[2][2] = {{{0}, {0}}, {{0}, {0}}};
#pragma unroll
  for (int kt = 0; kt < 10; ++kt) {
    if (kt < 8) WAIT_VM_BAR(16); else if (kt == 8) WAIT_VM_BAR(8); else WAIT_VM_BAR(0);
    if (kt + 3 < 10) stage(kt + 3);
    lptr sb = lds + (kt % G1_NS) * G1_STAGE;
#pragma unroll
    for (int ks = 0; ks < 4; ++ks) {
      bf16x8 xf[2], wf[2];
#pragma unroll
      for (int t = 0; t < 2; ++t) {
        xf[t] = *(LAS bf16x8*)(sb + xa[t] + (((2 * ks + h) ^ xsw) << 4));
        wf[t] = cat8(tr_read(sb + wa[t] + ks * 4096), tr_read(sb + wa[t] + ks * 4096 + 1024));
      }
#pragma unroll
      for (int a = 0; a < 2; ++a)
#pragma unroll
        for (int b = 0; b < 2; ++b) acc[a][b] = VT ? MFMA32(xf[a], wf[b], acc[a][b]) : MFMA32(wf[b], xf[a], acc[a][b]);
    }
  }
  if (VT) {
#pragma unroll
    for (int b = 0; b < 2; ++b) {
      const int f = (ng - 10) * 128 + wn * 64 + b * 32 + r, head = f / 20, dd = f - head * 20;
#pragma unroll
      for (int a = 0; a < 2; ++a) {
        const int tile = mt * 4 + wm * 2 + a;
        char* dst = ws + WS_KV + (size_t)head * KV_HEAD + (tile >> 1) * KV_PAIR + 2560 + (tile & 1) * 1280 + h * 320 + dd * 16;
#pragma unroll
        for (int s = 0; s < 2; ++s) {
          const f32x16& c = acc[a][b];
          *(u32x4*)(dst + s * 640) = (u32x4){pk_bf16(c[8 * s], c[8 * s + 1]), pk_bf16(c[8 * s + 2], c[8 * s + 3]),
                                            pk_bf16(c[8 * s + 4], c[8 * s + 5]), pk_bf16(c[8 * s + 6], c[8 * s + 7])};
        }
      }
    }
  } else {
    const bool isq = ng < 5;
    const float sc = isq ? QSCALE : 1.0f;
#pragma unroll
    for (int b = 0; b < 2; ++b)
#pragma unroll
      for (int g = 0; g < 4; ++g) {
        const int qi = (isq ? ng : ng - 5) * 32 + wn * 16 + b * 8 + 2 * g + h, head = qi / 5, qd = qi - head * 5;
#pragma unroll
        for (int a = 0; a < 2; ++a) {
          const int tile = mt * 4 + wm * 2 + a;
          size_t off;
          if (isq) off = WS_Q + (size_t)head * Q_HEAD + tile * Q_TILE + (qd < 4 ? (qd >> 1) * 512 + r * 16 + (qd & 1) * 8 : 1024 + r * 8);
          else off = WS_KV + (size_t)head * KV_HEAD + (tile >> 1) * KV_PAIR +
                     (qd < 4 ? (tile & 1) * 1024 + (qd >> 1) * 512 + r * 16 + (qd & 1) * 8 : 2048 + (tile & 1) * 256 + r * 8);
          const f32x16& c = acc[a][b];
          *(u32x2*)(ws + off) = (u32x2){pk_bf16(c[4 * g] * sc, c[4 * g + 1] * sc), pk_bf16(c[4 * g + 2] * sc, c[4 * g + 3] * sc)};
        }
      }
  }
}

__global__ void __launch_bounds__(256) qkv_kernel(const uint16_t* __restrict__ x, const uint16_t* __restrict__ wq, char* ws) {
  __shared__ __attribute__((aligned(16))) char smem[G1_NS * G1_STAGE];
  const int xcd = blockIdx.x & 7, idx = blockIdx.x >> 3;
  const int mt = (xcd >> 1) * 4 + (idx & 3), ng = (xcd & 1) * 8 + (idx >> 2);
  if (ng >= 15) return;
  if (ng >= 10) qkv_tile<true>(x, wq, ws, (lptr)smem, mt, ng);
  else qkv_tile<false>(x, wq, ws, (lptr)smem, mt, ng);
}

constexpr int AT_SLOT = 8 * KV_PAIR;

__device__ __forceinline__ f32x16 attn_exact(const char* kv, const char* qt) {
  const int lane = threadIdx.x & 63, r = lane & 31, h = lane >> 5;
  const bf16x8 qa = *(const bf16x8*)(qt + lane * 16);
  s16x4 qb = *(const s16x4*)(qt + 1024 + r * 8);
  if (h) qb = (s16x4){0, 0, 0, 0};
  f32x16 o = {0};
  float m = -INFINITY;
  const int rv = r < 20 ? r : 0;
  for (int t = 0; t < 64; ++t) {
    const char* pb = kv + (t >> 1) * KV_PAIR;
    const bf16x8 ka = *(const bf16x8*)(pb + (t & 1) * 1024 + lane * 16);
    s16x4 kb = *(const s16x4*)(pb + 2048 + (t & 1) * 256 + r * 8);
    if (h) kb = (s16x4){0, 0, 0, 0};
    u32x4 v0 = *(const u32x4*)(pb + 2560 + (t & 1) * 1280 + h * 320 + rv * 16);
    u32x4 v1 = *(const u32x4*)(pb + 2560 + (t & 1) * 1280 + 640 + h * 320 + rv * 16);
    if (r >= 20) { v0 = (u32x4){0x3f803f80u, 0x3f803f80u, 0x3f803f80u, 0x3f803f80u}; v1 = v0; }
    f32x16 s = {0};
    s = __builtin_amdgcn_mfma_f32_32x32x8bf16_1k(kb, qb, s, 0, 0, 0);
    s = MFMA32(ka, qa, s);
    float tm = s[0];
#pragma unroll
    for (int i = 1; i < 16; ++i) tm = fmaxf(tm, s[i]);
    tm = fmaxf(tm, __shfl_xor(tm, 32));
    const float mn = fmaxf(m, tm);
    const float ms = mn == -INFINITY ? 0.f : mn;
    const float al = __builtin_amdgcn_exp2f(m - ms);
    m = mn;
    f32x8 p0, p1;
#pragma unroll
    for (int i = 0; i < 8; ++i) { p0[i] = __builtin_amdgcn_exp2f(s[i] - ms); p1[i] = __builtin_amdgcn_exp2f(s[8 + i] - ms); }
#pragma unroll
    for (int i = 0; i < 16; ++i) o[i] *= al;
    o = __builtin_amdgcn_smfmac_f32_32x32x32_bf16(__builtin_bit_cast(bf16x8, v0), __builtin_bit_cast(bf16x16, p0), o, 0xdddd, 0, 0);
    o = __builtin_amdgcn_smfmac_f32_32x32x32_bf16(__builtin_bit_cast(bf16x8, v1), __builtin_bit_cast(bf16x16, p1), o, 0xdddd, 0, 0);
  }
  return o;
}

#define V8(p) "v" #p "0", "v" #p "1", "v" #p "2", "v" #p "3", "v" #p "4", "v" #p "5", "v" #p "6", "v" #p "7", "v" #p "8", "v" #p "9"

__global__ void __launch_bounds__(512, 2) attn_kernel(char* ws) {
  __shared__ __attribute__((aligned(16))) char smem[3 * AT_SLOT];
  const int tid = threadIdx.x, lane = tid & 63, r = lane & 31, h = lane >> 5;
  const int w = __builtin_amdgcn_readfirstlane(tid >> 6);
  const int head = (blockIdx.x & 7) * 4 + ((blockIdx.x >> 3) & 3), tile = (blockIdx.x >> 5) * 8 + w;
  const char* kv = ws + WS_KV + (size_t)head * KV_HEAD;
  const char* qt = ws + WS_Q + (size_t)head * Q_HEAD + tile * Q_TILE;
  const unsigned lb = (unsigned)(size_t)(lptr)smem;
  unsigned ak = lb + lane * 16, ab = lb + 2048 + lane * 8, av = lb + 2560 + h * 320 + (r < 20 ? r : 0) * 16;
  const unsigned goff = w * 1024 + lane * 16, m0b = lb + w * 1024;
  const unsigned qo1 = lane * 16, qo2 = 1024 + r * 8;
  const unsigned long long vmask = 0x000fffff000fffffull;
  f32x16 o = {0};
  unsigned s_m0, s_cnt, s_dst;
  asm volatile(
      ".macro at_exp b, i0, n\n"
      "  .set at_i, \\i0\n"
      "  .rept \\n\n"
      "    v_exp_f32 v[\\b+at_i], v[\\b+at_i]\n"
      "    .set at_i, at_i+1\n"
      "  .endr\n"
      ".endm\n"
      ".macro at_dma\n"
      "  .rept 5\n"
      "    s_mov_b32 m0, %[dst]\n"
      "    s_add_u32 %[dst], %[dst], 0x2000\n"
      "    global_load_lds_dwordx4 %[goff], s[40:41]\n"
      "    s_add_u32 s40, s40, 0x2000\n"
      "    s_addc_u32 s41, s41, 0\n"
      "  .endr\n"
      ".endm\n"
      ".macro at_step X, Y, VC, VN, kreg, breg, koff, vreg, voff\n"
      "  at_exp \\X, 0, 3\n"
      "  v_mfma_f32_32x32x4_2b_bf16 v[\\Y:\\Y+31], v[168:169], v[174:175], 0\n"
      "  ds_read_b64 v[168:169], \\breg offset:\\koff\n"
      "  s_mov_b64 exec, %[vmask]\n"
      "  ds_read_b128 v[\\VN:\\VN+3], \\vreg offset:\\voff\n"
      "  ds_read_b128 v[\\VN+4:\\VN+7], \\vreg offset:\\voff+640\n"
      "  ds_read_b128 v[\\VN+8:\\VN+11], \\vreg offset:\\voff+1280\n"
      "  ds_read_b128 v[\\VN+12:\\VN+15], \\vreg offset:\\voff+1920\n"
      "  s_mov_b64 exec, -1\n"
      "  at_exp \\X, 3, 8\n"
      "  v_smfmac_f32_32x32x32_bf16 %[o], v[\\VC:\\VC+3], v[\\X:\\X+7], v176\n"
      "  at_exp \\X, 11, 4\n"
      "  v_mfma_f32_32x32x16_bf16 v[\\Y:\\Y+15], v[160:163], v[170:173], v[\\Y:\\Y+15]\n"
      "  ds_read_b128 v[160:163], \\kreg offset:\\koff\n"
      "  at_exp \\X, 15, 4\n"
      "  v_smfmac_f32_32x32x32_bf16 %[o], v[\\VC+4:\\VC+7], v[\\X+8:\\X+15], v176\n"
      "  at_exp \\X, 19, 4\n"
      "  s_waitcnt lgkmcnt(6)\n"
      "  v_mfma_f32_32x32x16_bf16 v[\\Y+16:\\Y+31], v[164:167], v[170:173], v[\\Y+16:\\Y+31]\n"
      "  at_exp \\X, 23, 4\n"
      "  v_smfmac_f32_32x32x32_bf16 %[o], v[\\VC+8:\\VC+11], v[\\X+16:\\X+23], v176\n"
      "  at_exp \\X, 27, 5\n"
      "  ds_read_b128 v[164:167], \\kreg offset:\\koff+1024\n"
      "  s_waitcnt lgkmcnt(1)\n"
      "  v_smfmac_f32_32x32x32_bf16 %[o], v[\\VC+12:\\VC+15], v[\\X+24:\\X+31], v176\n"
      ".endm\n"
      "s_mov_b32 %[sm0], m0\n"
      "s_mov_b64 s[40:41], %[kv]\n"
      "s_mov_b32 %[dst], %[m0b]\n"
      "global_load_dwordx4 v[170:173], %[qo1], %[qt]\n"
      "global_load_dwordx2 v[174:175], %[qo2], %[qt]\n"
      "at_dma\n"
      "at_dma\n"
      "v_mov_b32 v176, 0xdddd\n"
      ".set at_i, 128\n"
      ".rept 32\n"
      "  v_mov_b32 v[at_i], 0x3f803f80\n"
      "  .set at_i, at_i+1\n"
      ".endr\n"
      "v_add_u32 v177, 40960, %[ak]\n"
      "v_add_u32 v178, 40960, %[ab]\n"
      "v_add_u32 v179, 40960, %[av]\n"
      "s_mov_b32 %[cnt], 0\n"
      "s_waitcnt vmcnt(5)\n"
      "s_barrier\n"
      "ds_read_b64 v[168:169], %[ab]\n"
      "ds_read_b128 v[160:163], %[ak]\n"
      "ds_read_b128 v[164:167], %[ak] offset:1024\n"
      "s_mov_b64 exec, %[vmask]\n"
      "ds_read_b128 v[128:131], %[av]\n"
      "ds_read_b128 v[132:135], %[av] offset:640\n"
      "ds_read_b128 v[136:139], %[av] offset:1280\n"
      "ds_read_b128 v[140:143], %[av] offset:1920\n"
      "s_mov_b64 exec, -1\n"
      "s_waitcnt lgkmcnt(4)\n"
      "v_mfma_f32_32x32x4_2b_bf16 v[64:95], v[168:169], v[174:175], 0\n"
      "ds_read_b64 v[168:169], %[ab] offset:5120\n"
      "s_nop 15\n"
      "s_nop 1\n"
      "v_mfma_f32_32x32x16_bf16 v[64:79], v[160:163], v[170:173], v[64:79]\n"
      "ds_read_b128 v[160:163], %[ak] offset:5120\n"
      "v_mfma_f32_32x32x16_bf16 v[80:95], v[164:167], v[170:173], v[80:95]\n"
      "ds_read_b128 v[164:167], %[ak] offset:6144\n"
      "s_nop 11\n"
      "s_waitcnt lgkmcnt(1)\n"
      "1:\n"
      "at_step 64, 96, 128, 144, %[ak], %[ab], 10240, %[av], 5120\n"
      "at_step 96, 64, 144, 128, %[ak], %[ab], 15360, %[av], 10240\n"
      "at_step 64, 96, 128, 144, %[ak], %[ab], 20480, %[av], 15360\n"
      "at_step 96, 64, 144, 128, %[ak], %[ab], 25600, %[av], 20480\n"
      "at_step 64, 96, 128, 144, %[ak], %[ab], 30720, %[av], 25600\n"
      "at_step 96, 64, 144, 128, %[ak], %[ab], 35840, %[av], 30720\n"
      "s_waitcnt vmcnt(0)\n"
      "s_barrier\n"
      "s_cmp_ge_u32 %[cnt], 2\n"
      "s_cbranch_scc1 2f\n"
      "at_dma\n"
      "s_mov_b32 %[dst], %[m0b]\n"
      "2:\n"
      "at_step 64, 96, 128, 144, v177, v178, 0, %[av], 35840\n"
      "at_step 96, 64, 144, 128, v177, v178, 5120, v179, 0\n"
      "v_mov_b32 %[ak], v177\n"
      "v_mov_b32 %[ab], v178\n"
      "v_mov_b32 %[av], v179\n"
      "s_add_u32 %[cnt], %[cnt], 1\n"
      "s_cmp_eq_u32 %[cnt], 2\n"
      "s_mov_b32 s42, 40960\n"
      "s_cselect_b32 s42, 0xfffec000, s42\n"
      "v_add_u32 v177, s42, v177\n"
      "v_add_u32 v178, s42, v178\n"
      "v_add_u32 v179, s42, v179\n"
      "s_cmp_lt_u32 %[cnt], 4\n"
      "s_cbranch_scc1 1b\n"
      "s_waitcnt lgkmcnt(0)\n"
      "s_mov_b32 m0, %[sm0]\n"
      "s_nop 15\n"
      ".purgem at_exp\n.purgem at_dma\n.purgem at_step\n"
      : [o] "+v"(o), [ak] "+v"(ak), [ab] "+v"(ab), [av] "+v"(av), [sm0] "=&s"(s_m0), [cnt] "=&s"(s_cnt), [dst] "=&s"(s_dst)
      : [kv] "s"(kv), [qt] "s"(qt), [goff] "v"(goff), [m0b] "s"(m0b), [qo1] "v"(qo1), [qo2] "v"(qo2), [vmask] "s"(vmask)
      : "memory", "scc", "s40", "s41", "s42", "v64", "v65", "v66", "v67", "v68", "v69", V8(7), V8(8), V8(9), V8(10), V8(11), V8(12), V8(13),
        V8(14), V8(15), V8(16), V8(17));
  float l = __shfl(o[8], r + 32);
  bool ok = l >= 0x1p-60f && l <= 0x1p60f;
#pragma unroll
  for (int i = 0; i < 12; ++i) ok = ok && (fabsf(o[i]) < INFINITY || (h && i >= 8));
  if (!__all(ok)) {
    o = attn_exact(kv, qt);
    l = __shfl(o[8], r + 32);
  }
  const float inv = 1.0f / l;
  char* ob = ws + WS_O + (size_t)tile * 40960;
  *(u32x4*)(ob + head * 1024 + lane * 16) = (u32x4){pk_bf16(o[0] * inv, o[1] * inv), pk_bf16(o[2] * inv, o[3] * inv),
                                                    pk_bf16(o[4] * inv, o[5] * inv), pk_bf16(o[6] * inv, o[7] * inv)};
  if (!h)
    *(u32x2*)(ob + (32 + (head >> 2)) * 1024 + ((head >> 1) & 1) * 512 + r * 16 + (head & 1) * 8) =
        (u32x2){pk_bf16(o[8] * inv, o[9] * inv), pk_bf16(o[10] * inv, o[11] * inv)};
}

__global__ void __launch_bounds__(256, 2) oproj_kernel(const char* __restrict__ ws, const uint16_t* __restrict__ wo, uint16_t* __restrict__ out) {
  __shared__ __attribute__((aligned(16))) char smem[81920];
  const int tid = threadIdx.x, lane = tid & 63, r = lane & 31, h = lane >> 5;
  const int w = __builtin_amdgcn_readfirstlane(tid >> 6);
  const int idx = blockIdx.x >> 3, tp = (blockIdx.x & 7) * 4 + (idx & 3), cg = idx >> 2;
  lptr lds = (lptr)smem;
  const unsigned lb = (unsigned)(size_t)lds;
  {
    const int rr = lane >> 3;
    const unsigned off = (unsigned)((160 * w + rr) * DM * 2 + cg * 128 + (((lane & 7) ^ (((rr >> 1) & 1) << 2)) << 4));
#pragma unroll
    for (int i = 0; i < 20; ++i) glds16((const char*)wo + i * 8 * DM * 2, off, lb + w * 20480 + i * 1024);
  }
  bf16x8 of[2][10];
#pragma unroll
  for (int k = 0; k < 10; ++k)
#pragma unroll
    for (int t = 0; t < 2; ++t) {
      const int ks = k < 8 ? 8 * w + k : 32 + 2 * w + (k - 8);
      of[t][k] = *(const bf16x8*)(ws + WS_O + (size_t)(tp * 2 + t) * 40960 + ks * 1024 + lane * 16);
    }
  WAIT_VM(0);
  const int q = (lane & 15) >> 2, p = lane & 3, blk = (lane >> 4) & 1, ps = ((p & 1) << 1) | (p >> 1);
  f32x16 acc[2][2] = {{{0}, {0}}, {{0}, {0}}};
#pragma unroll
  for (int k = 0; k < 10; ++k) {
    const int r0 = k < 8 ? 20 * k + 4 * h + q : 80 * (k - 8) + 40 * h + 16 + q, dr = k < 8 ? 8 : 20;
#pragma unroll
    for (int c = 0; c < 2; ++c) {
      lptr a = lds + w * 20480 + r0 * 128 + ((c ^ ((q >> 1) & 1)) << 6) + blk * 32 + ps * 8;
      const bf16x8 wf = cat8(tr_read(a), tr_read(a + dr * 128));
#pragma unroll
      for (int t = 0; t < 2; ++t) acc[t][c] = MFMA32(wf, of[t][k], acc[t][c]);
    }
  }
  __syncthreads();
  typedef float f32x4 __attribute__((ext_vector_type(4)));
#pragma unroll
  for (int j = 0; j < 4; ++j)
    if (j != w) {
      const f32x16& c = acc[j >> 1][j & 1];
#pragma unroll
      for (int g = 0; g < 4; ++g)
        *(LAS f32x4*)(lds + ((j * 4 + w) * 4 + g) * 1024 + lane * 16) = (f32x4){c[4 * g], c[4 * g + 1], c[4 * g + 2], c[4 * g + 3]};
    }
  __syncthreads();
  f32x16 s = w == 0 ? acc[0][0] : w == 1 ? acc[0][1] : w == 2 ? acc[1][0] : acc[1][1];
#pragma unroll
  for (int j = 0; j < 4; ++j)
    if (j != w) {
#pragma unroll
      for (int g = 0; g < 4; ++g) {
        const f32x4 v = *(LAS f32x4*)(lds + ((w * 4 + j) * 4 + g) * 1024 + lane * 16);
#pragma unroll
        for (int e = 0; e < 4; ++e) s[4 * g + e] += v[e];
      }
    }
  uint16_t* dst = out + (size_t)((tp * 2 + (w >> 1)) * 32 + r) * OUTW + DM + cg * 64 + (w & 1) * 32 + 8 * h;
#pragma unroll
  for (int t = 0; t < 2; ++t)
    *(u32x4*)(dst + 16 * t) = (u32x4){pk_bf16(s[8 * t], s[8 * t + 1]), pk_bf16(s[8 * t + 2], s[8 * t + 3]),
                                      pk_bf16(s[8 * t + 4], s[8 * t + 5]), pk_bf16(s[8 * t + 6], s[8 * t + 7])};
}

extern "C" void kernel_launch(void* const* d_in, const int* in_sizes, int n_in, void* d_out, int out_size, void* d_ws,
                              size_t ws_size, hipStream_t stream) {
  const uint16_t* const* in = (const uint16_t* const*)d_in;
  uint16_t* out = (uint16_t*)d_out;
  char* ws = (char*)d_ws;
  ssm_kernel<<<160, 256, 0, stream>>>(in[0], in[1], in[2], in[3], in[4], in[5], in[6], out);
  qkv_kernel<<<256, 256, 0, stream>>>(in[7], in[8], ws);
  attn_kernel<<<256, 512, 0, stream>>>(ws);
  oproj_kernel<<<320, 256, 0, stream>>>(ws, in[9], out);
}
```

```cpp
#include <hip/hip_runtime.h>
#include <hip/hip_bf16.h>
#include <stdint.h>

typedef __bf16 bf16x8 __attribute__((ext_vector_type(8)));
typedef __bf16 bf16x16 __attribute__((ext_vector_type(16)));
typedef __bf16 bf16x2 __attribute__((ext_vector_type(2)));
typedef _Float16 f16x8 __attribute__((ext_vector_type(8)));
typedef _Float16 f16x2 __attribute__((ext_vector_type(2)));
typedef short s16x4 __attribute__((ext_vector_type(4)));
typedef float f32x2 __attribute__((ext_vector_type(2)));
typedef float f32x4 __attribute__((ext_vector_type(4)));
typedef float f32x8 __attribute__((ext_vector_type(8)));
typedef float f32x16 __attribute__((ext_vector_type(16)));
typedef unsigned u32x2 __attribute__((ext_vector_type(2)));
typedef unsigned u32x4 __attribute__((ext_vector_type(4)));
typedef unsigned long long u64;
#define F2L 0
#define UB 8
#define US0 0
#define UL0 -1
#define CODE_LINES 448u
#define LAS __attribute__((address_space(3)))
#define GAS __attribute__((address_space(1)))
typedef LAS char* lptr;

constexpr int DM = 640, OUTW = 1280;
constexpr unsigned V_PLANE = 384, KV_PAIR = 5632, KV_HEAD = 32 * KV_PAIR, Q_TILE = 1280, Q_HEAD = 64 * Q_TILE, O_TILE = 40960;
constexpr unsigned UT_BLK = DM * 64 + 128, YT_ROW = 4096 + 128;
constexpr unsigned WS_FL = 0, WS_F1 = 4096, WS_F3 = 8192, WS_F0 = 12288, WS_F2 = 16384;
constexpr unsigned WS_KV = 131072, WS_Q = WS_KV + 32 * KV_HEAD, WS_O = WS_Q + 32 * Q_HEAD, WS_UT = WS_O + 64 * O_TILE,
                   WS_YT = WS_UT + 64 * UT_BLK, WS_WF = WS_YT + DM * YT_ROW;
constexpr unsigned KC_COPY = 2368, KC_SZ = 4 * KC_COPY, U_BLK = 80, U_PAD = 34, U_SZ = (U_PAD + 64) * U_BLK, TROW = 272, TAB_SZ = 58 * TROW;
constexpr unsigned L_TODD = 65536, L_TEVEN = L_TODD + 3 * TAB_SZ, L_PF = 160256, L_MISC = 160768;
constexpr unsigned L_KC = 65536, L_Z = L_KC + 3 * KC_SZ, L_BOX = 98304, BOX_SZ = 4352, L_YS = L_BOX + 3 * BOX_SZ, YS_SZ = 4608,
                   L_U = 135168, L_YT = 135168, L_QA = 125184, L_QB = 161024;
constexpr unsigned L_O = 57344, L_OP = 135168, L_END = 163840;
static_assert(L_TEVEN + 3 * TAB_SZ <= L_PF && L_Z + 3 * 512 <= L_BOX && L_YS + 3 * YS_SZ <= 131072 && L_U + 3 * U_SZ <= L_PF, "LDS map");
constexpr float QSCALE = 0.22360679774997896f * 1.4426950408889634f;
constexpr unsigned SPIN_CAP = 0x8000;

struct QTab { u64 e[20]; };
constexpr QTab QTAB = {{
    0x0ef60dac0c620420ull, 0x0124056a0d0704c5ull, 0x026e06b40e51060full, 0x03b807fe0f9b0759ull, 0x00e20cc504830c20ull,
    0x0ffe0eb40d6a0528ull, 0x022c06720e0f05cdull, 0x037607bc0f590717ull, 0x00a004e60c830441ull, 0x01ea0dcd058b0d28ull,
    0x03f406d50e720630ull, 0x03d90fbc077a0f17ull, 0x0ce604a40c620420ull, 0x01a805ee0d8b0549ull, 0x02f20ed506930e30ull,
    0x03fc07dd0f7a0738ull, 0x0dee0ca404620c20ull, 0x016605ac0d490507ull, 0x02b006f60e930651ull, 0x03fa0fdd079b0f38ull}};

__device__ __forceinline__ unsigned pk_bf16(float lo, float hi) {
  bf16x2 v = {(__bf16)lo, (__bf16)hi};
  return __builtin_bit_cast(unsigned, v);
}
__device__ __forceinline__ u32x4 pk8(const f32x16& c, int o, float s) {
  return (u32x4){pk_bf16(c[o] * s, c[o + 1] * s), pk_bf16(c[o + 2] * s, c[o + 3] * s), pk_bf16(c[o + 4] * s, c[o + 5] * s),
                 pk_bf16(c[o + 6] * s, c[o + 7] * s)};
}
__device__ __forceinline__ u32x4 pk8(const f32x16& c, int o) {
  return (u32x4){pk_bf16(c[o], c[o + 1]), pk_bf16(c[o + 2], c[o + 3]), pk_bf16(c[o + 4], c[o + 5]), pk_bf16(c[o + 6], c[o + 7])};
}
__device__ __forceinline__ float bf_lo(unsigned w) { return __uint_as_float(w << 16); }
__device__ __forceinline__ float bf_hi(unsigned w) { return __uint_as_float(w & 0xffff0000u); }
__device__ __forceinline__ float rnd_bf(float x) { return (float)(__bf16)x; }
__device__ __forceinline__ int swap23(int r) { return (r & ~12) | ((r & 4) << 1) | ((r & 8) >> 1); }
__device__ __forceinline__ s16x4 tr_read(lptr p) { return __builtin_amdgcn_ds_read_tr16_b64_v4i16((LAS s16x4*)p); }
__device__ __forceinline__ u32x4 cat8(s16x4 a, s16x4 b) {
  const u32x2 x = __builtin_bit_cast(u32x2, a), y = __builtin_bit_cast(u32x2, b);
  return (u32x4){x[0], x[1], y[0], y[1]};
}
__device__ __forceinline__ void st16(char* base, unsigned off, u32x4 v) {
  asm volatile("global_store_dwordx4 %0, %1, %2 sc1\n\ts_nop 1" ::"v"(off), "v"(v), "s"(base) : "memory");
}
__device__ __forceinline__ void st16u(char* base, unsigned off, u32x4 v) {
  asm volatile("global_store_dwordx4 %0, %1, %2 sc1\n\ts_nop 1" ::"v"(off), "v"(v), "s"(base) : "memory");
}
__device__ __forceinline__ void st8(char* base, unsigned off, u32x2 v) {
  asm volatile("global_store_dwordx2 %0, %1, %2 sc1" ::"v"(off), "v"(v), "s"(base) : "memory");
}
__device__ __forceinline__ void dma16(const void* base, unsigned off, unsigned lds) {
  asm volatile("s_mov_b32 m0, %2\n\ts_nop 0\n\tglobal_load_lds_dwordx4 %0, %1 sc1" ::"v"(off), "s"(base), "s"(lds) : "memory");
}
__device__ __forceinline__ void dma4(const void* base, unsigned off, unsigned lds) {
  asm volatile("s_mov_b32 m0, %2\n\ts_nop 0\n\tglobal_load_lds_dword %0, %1" ::"v"(off), "s"(base), "s"(lds) : "memory");
}
__device__ __forceinline__ void wait_flags(const char* ws, unsigned off, u64 tag) {
#pragma unroll 1
  for (unsigned n = 0; n < SPIN_CAP; ++n) {
    const u64 v = __hip_atomic_load((GAS const u64*)(ws + off), __ATOMIC_RELAXED, __HIP_MEMORY_SCOPE_AGENT);
    if (__all(v == tag)) break;
    __builtin_amdgcn_s_sleep(1);
  }
  asm volatile("" ::: "memory");
}
#define BAR_LDS() asm volatile("s_waitcnt lgkmcnt(0)\n\ts_barrier" ::: "memory")
#define V10(p) "v" #p "0", "v" #p "1", "v" #p "2", "v" #p "3", "v" #p "4", "v" #p "5", "v" #p "6", "v" #p "7", "v" #p "8", "v" #p "9"

__device__ __forceinline__ f32x2 cmul(f32x2 a, f32x2 b) {
  f32x2 t, r;
  asm("v_pk_mul_f32 %0, %1, %2 op_sel_hi:[1,0]" : "=v"(t) : "v"(a), "v"(b));
  asm("v_pk_fma_f32 %0, %1, %2, %3 op_sel:[1,1,0] op_sel_hi:[0,1,1] neg_lo:[0,1,0]" : "=v"(r) : "v"(a), "v"(b), "v"(t));
  return r;
}
#define CM_MUL(r, a, b) "v_pk_mul_f32 " r ", " a ", " b " op_sel_hi:[1,0]\n\t"
#define CM_FMA(r, a, b) "v_pk_fma_f32 " r ", " a ", " b ", " r " op_sel:[1,1,0] op_sel_hi:[0,1,1] neg_lo:[0,1,0]\n\t"
__device__ __forceinline__ void cmul4(f32x2& a0, f32x2 b0, f32x2& a1, f32x2 b1, f32x2& a2, f32x2 b2, f32x2& a3, f32x2 b3) {
  f32x2 r0, r1, r2, r3;
  asm(CM_MUL("%0", "%4", "%8") CM_MUL("%1", "%5", "%9") CM_MUL("%2", "%6", "%10") CM_MUL("%3", "%7", "%11")
      CM_FMA("%0", "%4", "%8") CM_FMA("%1", "%5", "%9") CM_FMA("%2", "%6", "%10") CM_FMA("%3", "%7", "%11")
      : "=&v"(r0), "=&v"(r1), "=&v"(r2), "=&v"(r3)
      : "v"(a0), "v"(a1), "v"(a2), "v"(a3), "v"(b0), "v"(b1), "v"(b2), "v"(b3));
  a0 = r0; a1 = r1; a2 = r2; a3 = r3;
}
template <int CTRL, int RM>
__device__ __forceinline__ unsigned dpp_max(unsigned v) {
  return max(v, (unsigned)__builtin_amdgcn_update_dpp(0, (int)v, CTRL, RM, 0xf, false));
}
__device__ __forceinline__ float next_lane(float v, float last) {
  return __int_as_float(__builtin_amdgcn_update_dpp(__float_as_int(last), __float_as_int(v), 0x130, 0xf, 0xf, false));
}
__device__ __forceinline__ float add1(float a, float b) {
  float r;
  asm("v_add_f32 %0, %1, %2" : "=v"(r) : "v"(a), "v"(b));
  return r;
}
__device__ __forceinline__ float lane0(float v) { return __int_as_float(__builtin_amdgcn_readlane(__float_as_int(v), 0)); }
__device__ __forceinline__ f32x2 cexp_p(float al2, float rev, float p) {
  const float m = __builtin_amdgcn_exp2f(al2 * p);
  const float f = __builtin_amdgcn_fractf(rev * p);
  return (f32x2){m * __builtin_amdgcn_cosf(f), m * __builtin_amdgcn_sinf(f)};
}
__device__ __forceinline__ void tab_put(lptr t, int row, f32x2 v) {
  const f16x2 hv = {(_Float16)v[0], (_Float16)v[1]};
  *(LAS unsigned*)(t + row * TROW) = __builtin_bit_cast(unsigned, hv);
}
__device__ __forceinline__ void gen_tables(lptr tw, float la, float ai, float cr, float ci, float dt, int ex) {
  const float are = -rnd_bf(expf(la)) * dt, aim = ai * dt;
  const float al2 = are * 1.4426950408889634f, rev = aim * 0.15915494309189535f;
  const f32x2 w1 = cexp_p(al2, rev, 1.0f), W = cexp_p(al2, rev, 256.0f);
  f32x2 fb = cexp_p(al2, rev, 8.0f);
  fb[1] = -fb[1];
  f32x2 e0 = {ldexpf(cr, -ex), ldexpf(ci, -ex)}, w2 = w1, fc = fb, e1 = e0, g = e0;
  cmul4(w2, w1, fc, fb, e1, w1, g, W);
  f32x2 w4 = w2, fa = fc, fd = fc, g2 = g, e2 = g;
  cmul4(w4, w2, fa, fc, fd, fb, g2, W);
  const f32x2 f4 = fa;
  tab_put(tw, 26, (f32x2){1.0f, 0.0f}); tab_put(tw, 27, fb); tab_put(tw, 28, fc); tab_put(tw, 29, fd); tab_put(tw, 30, fa);
  tab_put(tw, 0, e0); tab_put(tw, 1, e1); tab_put(tw, 11, g); tab_put(tw, 17, g2);
  g = g2;
  f32x2 g768 = g;
#pragma unroll
  for (int k = 1; k < 7; ++k) {
    cmul4(fb, f4, fc, f4, fd, f4, fa, f4);
    tab_put(tw, 27 + 4 * k, fb); tab_put(tw, 28 + 4 * k, fc); tab_put(tw, 29 + 4 * k, fd); tab_put(tw, 30 + 4 * k, fa);
    if (k < 5) {
      cmul4(g, W, e0, w2, e2, w2, e1, w2);
      tab_put(tw, k == 1 ? 19 : 19 + k, g); tab_put(tw, 2 * k, e0); tab_put(tw, 11 + k, e2); tab_put(tw, 2 * k + 1, e1);
    } else if (k == 5) {
      cmul4(g, W, e0, w2, e2, w2, g2, w4);
      tab_put(tw, 24, g); tab_put(tw, 10, e0); tab_put(tw, 16, e2); tab_put(tw, 18, g2);
    }
    if (k == 1) g768 = g;
  }
  cmul4(fb, f4, fc, f4, fd, f4, g, W);
  tab_put(tw, 55, fb); tab_put(tw, 56, fc); tab_put(tw, 57, fd); tab_put(tw, 25, g);
  tab_put(tw, 20, cmul(g768, w4));
}
__device__ __forceinline__ int c_exp(unsigned cr, unsigned ci) {
  unsigned mx = max(max(cr & 0x7fffu, ci & 0x7fffu), max((cr >> 16) & 0x7fffu, (ci >> 16) & 0x7fffu));
  mx = dpp_max<0x111, 0xf>(mx); mx = dpp_max<0x112, 0xf>(mx); mx = dpp_max<0x114, 0xf>(mx); mx = dpp_max<0x118, 0xf>(mx);
  mx = dpp_max<0x142, 0xa>(mx); mx = dpp_max<0x143, 0xc>(mx);
  return (__builtin_amdgcn_readlane(mx, 63) >> 7) - 126;
}
__device__ __forceinline__ void put_run(lptr kc, int p, float v0, float v1, float v2, float v3, float v4, float v5, float v6,
                                        bool c0, bool c123) {
  const unsigned d32 = pk_bf16(v3, v2), d10 = pk_bf16(v1, v0), d65 = pk_bf16(v6, v5), d43 = pk_bf16(v4, v3),
                 d54 = pk_bf16(v5, v4), d21 = pk_bf16(v2, v1);
  if (c0) *(LAS u32x2*)(kc + 2 * (1084 - p)) = (u32x2){d32, d10};
  if (c123) {
    lptr q = kc + 2 * (1080 - p);
    *(LAS u32x2*)(q + KC_COPY) = (u32x2){d65, d43};
    *(LAS u32x2*)(q + 2 * KC_COPY) = (u32x2){d54, d32};
    *(LAS u32x2*)(q + 3 * KC_COPY) = (u32x2){d43, d21};
  }
}
__global__ void __launch_bounds__(512, 2)
fused_kernel(const uint16_t* __restrict__ u, const uint16_t* __restrict__ logA, const uint16_t* __restrict__ Aim,
             const uint16_t* __restrict__ Cre, const uint16_t* __restrict__ Cim, const uint16_t* __restrict__ logdt,
             const uint16_t* __restrict__ Dsk, const uint16_t* __restrict__ x, const uint16_t* __restrict__ wq,
             const uint16_t* __restrict__ wo, uint16_t* __restrict__ out, char* ws, const QTab qtab) {
  __shared__ __attribute__((aligned(16))) char smem[L_END];
  asm volatile("v_readfirstlane_b32 s44, %0\n\tv_lshlrev_b32 v64, 5, %0\n\tv_and_b32 v64, 0x1e0, v64\n\ts_cmp_lt_u32 s44, 448\n\t"
               "s_cbranch_scc1 1f\n\ts_getpc_b64 s[46:47]\n\tv_add_u32 v64, %1, v64\n\tv_min_u32 v64, %2, v64\n\t"
               "v_lshlrev_b32 v64, 7, v64\n\ts_mov_b32 m0, %3\n\ts_mov_b64 exec, 0xffff\n\tglobal_load_lds_dword v64, s[46:47]\n\t"
               "s_mov_b64 exec, -1\n1:"
               ::"v"(threadIdx.x), "s"(blockIdx.x >> 3), "n"(CODE_LINES - 1), "n"(L_PF) : "memory", "scc", "s44", "s46", "s47", "v64");
  const int tid = threadIdx.x, lane = tid & 63, r = lane & 31, h = lane >> 5;
  const int w = __builtin_amdgcn_readfirstlane(tid >> 6);
  const int xcd = blockIdx.x & 7, idx = blockIdx.x >> 3;
  const int mt = (xcd >> 1) * 4 + (idx & 3), ng = (xcd & 1) * 8 + (idx >> 2);
  const int head = xcd * 4 + (idx & 3), qb = idx >> 2;
  const int odd = idx & 1, d5 = 80 * xcd + 5 * (idx >> 1);
  const int xp = xcd >> 1, rb = (xcd & 1) * 32 + idx;
  const bool gemm = ng < 15, vt = ng >= 10, isq = ng < 5;
  const int g5 = vt ? 0 : isq ? ng : ng - 5;
  u64 qe0 = qtab.e[g5 * 4 + 2 * (w & 1)], qe1 = qtab.e[g5 * 4 + 2 * (w & 1) + 1];
  asm volatile("" : "+s"(qe0), "+s"(qe1), "+s"(u), "+s"(logA), "+s"(Aim), "+s"(Cre), "+s"(Cim), "+s"(logdt), "+s"(Dsk), "+s"(x),
                    "+s"(wq), "+s"(wo), "+s"(out), "+s"(ws));
  lptr lds = (lptr)smem;
  unsigned l16 = lane * 16, r8 = r * 8;
  asm volatile("" : "+v"(l16), "+v"(r8));
  const unsigned m0b = w * 4096;
  int dl;
  unsigned ka, ua, yo, po1, f1, f2, f3, ost, ostb, av, ys, tw, tr, gy;
  const char *kv, *qt, *wf, *ot;
  const int tile = qb * 8 + w, tt = qb * 8 + xcd, cg = idx & 3;
  auto later = [&]() {
    dl = w < 4 ? w >> 1 : 2;
    const int hi = w < 4 ? w & 1 : 1 - odd, mtop = w < 4 ? 33 : w < 7 ? 8 * (w - 4) + 9 : 33;
    ka = L_KC + dl * KC_SZ + ((3 - r) & 3) * KC_COPY + 2 * (1084 + 8 * h - 4 * (r >> 2)) - 64 * mtop;
    ua = L_U + dl * U_SZ + (U_PAD + 32 * hi + r - mtop) * U_BLK + 16 * h;
    yo = WS_YT + (dl == 2 ? d5 + 2 : d5 + 3 * odd + dl) * YT_ROW + hi * 2048 + l16;
    ys = L_YS + dl * YS_SZ + r * 144 + 16 * h;
    tw = L_YT + w * 2560 + r * U_BLK + 8 * h;
    tr = L_YT + w * 2560 + (lane >> 2) * U_BLK + (lane & 3) * 16;
    gy = __builtin_amdgcn_readfirstlane(w < 4 ? 2 * hi : w < 7 ? 1 : 4 + 2 * hi);
    const int l8 = lane >> 3, nsel = ((l8 < 4 ? l8 : lane) & 1) ? (20 * head + 19) >> 7 : (20 * head) >> 7;
    po1 = WS_F1 + (l8 == 4 ? 15 : nsel + (l8 < 2 ? 5 : l8 < 4 ? 10 : 0)) * 128 + (l8 < 5 ? lane & 7 : qb) * 16;
    f1 = WS_F1 + (ng * 16 + mt) * 8; f3 = WS_F3 + (xcd * 32 + idx) * 8; f2 = F2L ? WS_F2 + (tile * 8 + xcd) * 128 + (idx & 3) * 8 : WS_F2 + (tile * 32 + head) * 8;
    kv = ws + WS_KV + head * KV_HEAD;
    qt = ws + WS_Q + head * Q_HEAD + (qb * 8 + 2 * (w & 3)) * Q_TILE;
    wf = ws + WS_WF + (cg * 200 + 25 * w) * 1024;
    ot = ws + WS_O + tt * O_TILE;
    ost = WS_O + tile * O_TILE + head * 1024; ostb = WS_O + tile * O_TILE + 32768 + head * 256;
    av = 2560 + h * V_PLANE + (r < 23 ? r : 23) * 16;
    asm volatile("" : "+{v20}"(ka), "+{v21}"(ua), "+{v22}"(yo), "+{v23}"(po1), "+{v24}"(av), "+{v25}"(ys), "+{v26}"(tw), "+{v27}"(tr));
    asm volatile("" : "+s"(dl), "+s"(f1), "+s"(f2), "+s"(f3), "+s"(kv), "+s"(qt), "+s"(wf), "+s"(ot), "+s"(ost), "+s"(ostb), "+s"(gy));
  };
#define BAR() asm volatile("s_barrier" ::: "memory")

  const int xr0 = lane >> 3, kr0 = lane >> 4;
  const unsigned p0 = 4096 + (mt * 128 + xr0) * 1280 + (((lane & 7) ^ (xr0 >> 1)) << 4);
  const unsigned p1 = 4096 - 1024 + (mt * 128 + xr0 + 8) * 1280 + (((lane & 7) ^ ((xr0 >> 1) + 4)) << 4);
  const unsigned p2 = 4096 - 2048 + kr0 * 3840 + ng * 256 + (((lane & 15) ^ (kr0 << 2)) << 4);
  const unsigned p3 = p2 - 1024 + 4 * 3840;
  const unsigned kt0 = ((idx >> 2) + 3 * (idx & 3)) % 10, kt1 = kt0 == 9 ? 0 : kt0 + 1, kt2 = kt1 == 9 ? 0 : kt1 + 1;
  const u64 xb = (u64)x - 4096, wb = (u64)wq - 4096;
  const int dk = (w & 3) == 2 ? d5 + 2 : d5 + 3 * odd + (w & 1);
  if (w < 4) {
    const unsigned tbo = L_TODD + w * TAB_SZ;
    if (w < 3) {
      dma4(logA + dk * 128, lane * 4, tbo); dma4(Aim + dk * 128, lane * 4, tbo + 256);
      dma4(Cre + dk * 128, lane * 4, tbo + 512); dma4(Cim + dk * 128, lane * 4, tbo + 768);
      dma4(logdt + (dk & ~1), 0, tbo + 1024);
    }
    BAR();
    auto odd_tables = [&]() {
      lptr tb = lds + tbo;
      const unsigned la = *(LAS unsigned*)(tb + 4 * lane), ai = *(LAS unsigned*)(tb + 256 + 4 * lane),
                     cr = *(LAS unsigned*)(tb + 512 + 4 * lane), ci = *(LAS unsigned*)(tb + 768 + 4 * lane),
                     ld = *(LAS uint16_t*)(tb + 1024 + 2 * (dk & 1));
      gen_tables(tb + 4 * lane, bf_hi(la), bf_hi(ai), bf_hi(cr), bf_hi(ci), rnd_bf(expf(bf_lo(ld))), c_exp(cr, ci));
    };
    if (gemm) {
      const unsigned o0 = p0 + w * 32 * 1280, o1 = p1 + w * 32 * 1280, o2 = p2 + w * 16 * 3840, o3 = p3 + w * 16 * 3840;
      const unsigned o4 = o0 + 16 * 1280, o5 = o1 + 16 * 1280, o6 = o2 + 8 * 3840, o7 = o3 + 8 * 3840, m0g = w * 8192;
#define TILE(m0_, a_, b_, c_, d_, xs_, ws_)                                                                               \
  asm volatile("s_mov_b32 m0, %0\n\ts_nop 0\n\tglobal_load_lds_dwordx4 %1, %5\n\tglobal_load_lds_dwordx4 %2, %5 offset:1024\n\t" \
               "global_load_lds_dwordx4 %3, %6 offset:2048\n\tglobal_load_lds_dwordx4 %4, %6 offset:3072"                 \
               ::"s"(m0_), "v"(a_), "v"(b_), "v"(c_), "v"(d_), "s"(xs_), "s"(ws_) : "memory")
      TILE(m0g, o0, o1, o2, o3, xb + kt0 * 128, wb + kt0 * 245760ull);
      TILE(4096 + m0g, o4, o5, o6, o7, xb + kt0 * 128, wb + kt0 * 245760ull);
      unsigned kt = kt2 == 9 ? 0 : kt2 + 1;
      u64 xs = xb + kt * 128, wsrc = wb + kt * 245760ull;
      const int wm = w >> 1, xr = vt ? swap23(r) : r, sw = (xr >> 1) & 7;
      const unsigned xa = (4 * wm + (xr >> 4)) * 4096 + ((xr >> 3) & 1) * 1024 + (xr & 7) * 128;
      unsigned xa0 = xa + ((h ^ sw) << 4), xa1 = xa + (((2 + h) ^ sw) << 4), xa2 = xa + (((4 + h) ^ sw) << 4), xa3 = xa + (((6 + h) ^ sw) << 4);
      const int tile0 = mt * 4 + wm * 2;
      const int q = (lane & 15) >> 2, blk = (lane >> 4) & 1, pp = lane & 3;
      unsigned wa[2], da[2], db[2], dc[2];
      f32x16 c00, c01, c10, c11;
#define QKV_OPS                                                                                                          \
      : "=&{v[64:79]}"(c00), "=&{v[80:95]}"(c01), "=&{v[96:111]}"(c10), "=&{v[112:127]}"(c11), [x0] "+&v"(xa0),         \
        [x1] "+&v"(xa1), [x2] "+&v"(xa2), [x3] "+&v"(xa3), [wa0] "+&v"(wa[0]), [wa1] "+&v"(wa[1]), [kt] "+&s"(kt),      \
        "+&{s[40:41]}"(xs), "+&{s[42:43]}"(wsrc)     \
      : [o0] "v"(o0), [o1] "v"(o1), [o2] "v"(o2), [o3] "v"(o3), [o4] "v"(o4), [o5] "v"(o5), [o6] "v"(o6), [o7] "v"(o7), \
        [m0g] "s"(m0g) \
      : "memory", "scc", "s44", "s45", "s46", "s47", "s48", "s49", "v164", "v165", "v166", "v167", "v168", "v169", V10(17), V10(18), "v190", "v191", "v192", "v193", \
        "v194", "v195"
      auto tile1 = [&]() {
        asm volatile(
      "s_mov_b64 s[40:41], %[xs]\n"
      "s_mov_b64 s[42:43], %[wr]\n"
      "s_mov_b32 m0, 32768\n"
      "s_nop 0\n"
      "global_load_lds_dwordx4 %[p0], s[40:41] offset:0\n"
      "global_load_lds_dwordx4 %[p1], s[40:41] offset:1024\n"
      "global_load_lds_dwordx4 %[p2], s[42:43] offset:2048\n"
      "global_load_lds_dwordx4 %[p3], s[42:43] offset:3072\n"
      "s_add_u32 s40, s40, 20480\n"
      "s_addc_u32 s41, s41, 0\n"
      "s_add_u32 s42, s42, 30720\n"
      "s_addc_u32 s43, s43, 0\n"
      "s_mov_b32 m0, 36864\n"
      "s_nop 0\n"
      "global_load_lds_dwordx4 %[p0], s[40:41] offset:0\n"
      "global_load_lds_dwordx4 %[p1], s[40:41] offset:1024\n"
      "global_load_lds_dwordx4 %[p2], s[42:43] offset:2048\n"
      "global_load_lds_dwordx4 %[p3], s[42:43] offset:3072\n"
      "s_add_u32 s40, s40, 20480\n"
      "s_addc_u32 s41, s41, 0\n"
      "s_add_u32 s42, s42, 30720\n"
      "s_addc_u32 s43, s43, 0\n"
      "s_mov_b32 m0, 40960\n"
      "s_nop 0\n"
      "global_load_lds_dwordx4 %[p0], s[40:41] offset:0\n"
      "global_load_lds_dwordx4 %[p1], s[40:41] offset:1024\n"
      "global_load_lds_dwordx4 %[p2], s[42:43] offset:2048\n"
      "global_load_lds_dwordx4 %[p3], s[42:43] offset:3072\n"
      "s_add_u32 s40, s40, 20480\n"
      "s_addc_u32 s41, s41, 0\n"
      "s_add_u32 s42, s42, 30720\n"
      "s_addc_u32 s43, s43, 0\n"
      "s_mov_b32 m0, 45056\n"
      "s_nop 0\n"
      "global_load_lds_dwordx4 %[p0], s[40:41] offset:0\n"
      "global_load_lds_dwordx4 %[p1], s[40:41] offset:1024\n"
      "global_load_lds_dwordx4 %[p2], s[42:43] offset:2048\n"
      "global_load_lds_dwordx4 %[p3], s[42:43] offset:3072\n"
      "s_add_u32 s40, s40, 20480\n"
      "s_addc_u32 s41, s41, 0\n"
      "s_add_u32 s42, s42, 30720\n"
      "s_addc_u32 s43, s43, 0\n"
      "s_mov_b32 m0, 49152\n"
      "s_nop 0\n"
      "global_load_lds_dwordx4 %[p0], s[40:41] offset:0\n"
      "global_load_lds_dwordx4 %[p1], s[40:41] offset:1024\n"
      "global_load_lds_dwordx4 %[p2], s[42:43] offset:2048\n"
      "global_load_lds_dwordx4 %[p3], s[42:43] offset:3072\n"
      "s_add_u32 s40, s40, 20480\n"
      "s_addc_u32 s41, s41, 0\n"
      "s_add_u32 s42, s42, 30720\n"
      "s_addc_u32 s43, s43, 0\n"
      "s_mov_b32 m0, 53248\n"
      "s_nop 0\n"
      "global_load_lds_dwordx4 %[p0], s[40:41] offset:0\n"
      "global_load_lds_dwordx4 %[p1], s[40:41] offset:1024\n"
      "global_load_lds_dwordx4 %[p2], s[42:43] offset:2048\n"
      "global_load_lds_dwordx4 %[p3], s[42:43] offset:3072\n"
      "s_add_u32 s40, s40, 20480\n"
      "s_addc_u32 s41, s41, 0\n"
      "s_add_u32 s42, s42, 30720\n"
      "s_addc_u32 s43, s43, 0\n"
      "s_mov_b32 m0, 57344\n"
      "s_nop 0\n"
      "global_load_lds_dwordx4 %[p0], s[40:41] offset:0\n"
      "global_load_lds_dwordx4 %[p1], s[40:41] offset:1024\n"
      "global_load_lds_dwordx4 %[p2], s[42:43] offset:2048\n"
      "global_load_lds_dwordx4 %[p3], s[42:43] offset:3072\n"
      "s_add_u32 s40, s40, 20480\n"
      "s_addc_u32 s41, s41, 0\n"
      "s_add_u32 s42, s42, 30720\n"
      "s_addc_u32 s43, s43, 0\n"
      "s_mov_b32 m0, 61440\n"
      "s_nop 0\n"
      "global_load_lds_dwordx4 %[p0], s[40:41] offset:0\n"
      "global_load_lds_dwordx4 %[p1], s[40:41] offset:1024\n"
      "global_load_lds_dwordx4 %[p2], s[42:43] offset:2048\n"
      "global_load_lds_dwordx4 %[p3], s[42:43] offset:3072\n"
      "s_mov_b32 m0, 160256\n"
      "s_nop 0\n"
      "global_load_lds_dword %[pf], %[wn]\n"
      "global_load_lds_dword %[pf], %[wn] offset:128\n"
          :
          : [p0] "v"(p0), [p1] "v"(p1), [p2] "v"(p2), [p3] "v"(p3), [xs] "s"(xb + kt1 * 128), [wr] "s"(wb + kt1 * 245760ull),
            [wn] "s"(wb + kt2 * 245760ull), [pf] "v"(4096 + lane * 3840 + ng * 256)
          : "memory", "scc", "s40", "s41", "s42", "s43");
      };
#define PRE_GEMM()                                                                                   \
  do {                                                                                               \
    asm volatile("" : "+v"(xa0), "+v"(xa1), "+v"(xa2), "+v"(xa3), "+v"(wa[0]), "+v"(wa[1]));        \
    if (w < 3) { asm volatile("s_waitcnt vmcnt(8)" ::: "memory"); odd_tables(); } else tile1();      \
    BAR_LDS();                                          \
    later();                                                                                         \
  } while (0)
      if (vt) {
#pragma unroll
        for (int b = 0; b < 2; ++b) {
          const int wn = 2 * (w & 1) + b, f = (ng - 10) * 128 + wn * 32 + r, hd = f / 20;
          wa[b] = h * 4096 + 2048 + q * 256 + ((wn ^ q) << 6) + blk * 32 + pp * 8;
          da[b] = WS_KV + hd * KV_HEAD + (tile0 >> 1) * KV_PAIR + 2560 + h * V_PLANE + (f - hd * 20) * 16;
          asm volatile("" : "+v"(da[b]));
        }
        PRE_GEMM();
        asm volatile(
      "s_mov_b32 s48, 128\n"
      "s_mov_b32 s49, 245760\n"
      "s_cmp_eq_u32 %[m0g], 24576\n"
      "s_cbranch_scc1 1f\n"
      "s_waitcnt vmcnt(0)\n"
      "1:\n"
      "s_waitcnt vmcnt(34)\n"
      "s_barrier\n"
      "ds_read_b128 v[164:167], %[x0]\n"
      "ds_read_b64_tr_b16 v[172:173], %[wa0] offset:0\n"
      "ds_read_b64_tr_b16 v[174:175], %[wa0] offset:1024\n"
      "ds_read_b64_tr_b16 v[176:177], %[wa1] offset:0\n"
      "ds_read_b64_tr_b16 v[178:179], %[wa1] offset:1024\n"
      "ds_read_b128 v[168:171], %[x0] offset:8192\n"
      "s_waitcnt lgkmcnt(3)\n"
      "v_mfma_f32_32x32x16_bf16 v[64:79], v[164:167], v[172:175], 0\n"
      "ds_read_b128 v[180:183], %[x1]\n"
      "ds_read_b64_tr_b16 v[188:189], %[wa0] offset:8192\n"
      "ds_read_b64_tr_b16 v[190:191], %[wa0] offset:9216\n"
      "s_waitcnt lgkmcnt(4)\n"
      "v_mfma_f32_32x32x16_bf16 v[80:95], v[164:167], v[176:179], 0\n"
      "ds_read_b64_tr_b16 v[192:193], %[wa1] offset:8192\n"
      "ds_read_b64_tr_b16 v[194:195], %[wa1] offset:9216\n"
      "ds_read_b128 v[184:187], %[x1] offset:8192\n"
      "s_waitcnt lgkmcnt(6)\n"
      "v_mfma_f32_32x32x16_bf16 v[96:111], v[168:171], v[172:175], 0\n"
      "v_mfma_f32_32x32x16_bf16 v[112:127], v[168:171], v[176:179], 0\n"
      "s_waitcnt lgkmcnt(3)\n"
      "v_mfma_f32_32x32x16_bf16 v[64:79], v[180:183], v[188:191], v[64:79]\n"
      "ds_read_b128 v[164:167], %[x2]\n"
      "ds_read_b64_tr_b16 v[172:173], %[wa0] offset:16384\n"
      "ds_read_b64_tr_b16 v[174:175], %[wa0] offset:17408\n"
      "s_waitcnt lgkmcnt(4)\n"
      "v_mfma_f32_32x32x16_bf16 v[80:95], v[180:183], v[192:195], v[80:95]\n"
      "ds_read_b64_tr_b16 v[176:177], %[wa1] offset:16384\n"
      "ds_read_b64_tr_b16 v[178:179], %[wa1] offset:17408\n"
      "ds_read_b128 v[168:171], %[x2] offset:8192\n"
      "s_waitcnt lgkmcnt(6)\n"
      "v_mfma_f32_32x32x16_bf16 v[96:111], v[184:187], v[188:191], v[96:111]\n"
      "v_mfma_f32_32x32x16_bf16 v[112:127], v[184:187], v[192:195], v[112:127]\n"
      "s_waitcnt lgkmcnt(3)\n"
      "v_mfma_f32_32x32x16_bf16 v[64:79], v[164:167], v[172:175], v[64:79]\n"
      "ds_read_b128 v[180:183], %[x3]\n"
      "ds_read_b64_tr_b16 v[188:189], %[wa0] offset:24576\n"
      "ds_read_b64_tr_b16 v[190:191], %[wa0] offset:25600\n"
      "s_waitcnt lgkmcnt(4)\n"
      "v_mfma_f32_32x32x16_bf16 v[80:95], v[164:167], v[176:179], v[80:95]\n"
      "ds_read_b64_tr_b16 v[192:193], %[wa1] offset:24576\n"
      "ds_read_b64_tr_b16 v[194:195], %[wa1] offset:25600\n"
      "ds_read_b128 v[184:187], %[x3] offset:8192\n"
      "s_waitcnt lgkmcnt(6)\n"
      "v_mfma_f32_32x32x16_bf16 v[96:111], v[168:171], v[172:175], v[96:111]\n"
      "v_mfma_f32_32x32x16_bf16 v[112:127], v[168:171], v[176:179], v[112:127]\n"
      "s_waitcnt vmcnt(0) lgkmcnt(0)\n"
      "s_barrier\n"
      "s_mov_b32 s46, 0x8000\n"
      "v_add_u32 %[x0], s46, %[x0]\n"
      "v_add_u32 %[wa0], s46, %[wa0]\n"
      "v_add_u32 %[wa1], s46, %[wa1]\n"
      "v_mfma_f32_32x32x16_bf16 v[64:79], v[180:183], v[188:191], v[64:79]\n"
      "ds_read_b128 v[164:167], %[x0]\n"
      "ds_read_b64_tr_b16 v[172:173], %[wa0] offset:0\n"
      "ds_read_b64_tr_b16 v[174:175], %[wa0] offset:1024\n"
      "v_mfma_f32_32x32x16_bf16 v[80:95], v[180:183], v[192:195], v[80:95]\n"
      "ds_read_b64_tr_b16 v[176:177], %[wa1] offset:0\n"
      "ds_read_b64_tr_b16 v[178:179], %[wa1] offset:1024\n"
      "ds_read_b128 v[168:171], %[x0] offset:8192\n"
      "v_mfma_f32_32x32x16_bf16 v[96:111], v[184:187], v[188:191], v[96:111]\n"
      "s_add_u32 s44, %[m0g], 0\n"
      "s_mov_b32 m0, s44\n"
      "s_nop 0\n"
      "global_load_lds_dwordx4 %[o0], s[40:41] offset:0\n"
      "v_mfma_f32_32x32x16_bf16 v[112:127], v[184:187], v[192:195], v[112:127]\n"
      "global_load_lds_dwordx4 %[o1], s[40:41] offset:1024\n"
      "s_waitcnt lgkmcnt(3)\n"
      "v_add_u32 %[x1], s46, %[x1]\n"
      "v_mfma_f32_32x32x16_bf16 v[64:79], v[164:167], v[172:175], v[64:79]\n"
      "ds_read_b128 v[180:183], %[x1]\n"
      "ds_read_b64_tr_b16 v[188:189], %[wa0] offset:8192\n"
      "ds_read_b64_tr_b16 v[190:191], %[wa0] offset:9216\n"
      "s_waitcnt lgkmcnt(4)\n"
      "v_mfma_f32_32x32x16_bf16 v[80:95], v[164:167], v[176:179], v[80:95]\n"
      "ds_read_b64_tr_b16 v[192:193], %[wa1] offset:8192\n"
      "ds_read_b64_tr_b16 v[194:195], %[wa1] offset:9216\n"
      "ds_read_b128 v[184:187], %[x1] offset:8192\n"
      "s_waitcnt lgkmcnt(6)\n"
      "v_mfma_f32_32x32x16_bf16 v[96:111], v[168:171], v[172:175], v[96:111]\n"
      "global_load_lds_dwordx4 %[o2], s[42:43] offset:2048\n"
      "v_mfma_f32_32x32x16_bf16 v[112:127], v[168:171], v[176:179], v[112:127]\n"
      "global_load_lds_dwordx4 %[o3], s[42:43] offset:3072\n"
      "s_waitcnt lgkmcnt(3)\n"
      "v_add_u32 %[x2], s46, %[x2]\n"
      "v_mfma_f32_32x32x16_bf16 v[64:79], v[180:183], v[188:191], v[64:79]\n"
      "ds_read_b128 v[164:167], %[x2]\n"
      "ds_read_b64_tr_b16 v[172:173], %[wa0] offset:16384\n"
      "ds_read_b64_tr_b16 v[174:175], %[wa0] offset:17408\n"
      "s_waitcnt lgkmcnt(4)\n"
      "v_mfma_f32_32x32x16_bf16 v[80:95], v[180:183], v[192:195], v[80:95]\n"
      "ds_read_b64_tr_b16 v[176:177], %[wa1] offset:16384\n"
      "ds_read_b64_tr_b16 v[178:179], %[wa1] offset:17408\n"
      "ds_read_b128 v[168:171], %[x2] offset:8192\n"
      "s_waitcnt lgkmcnt(6)\n"
      "v_mfma_f32_32x32x16_bf16 v[96:111], v[184:187], v[188:191], v[96:111]\n"
      "s_add_u32 s44, %[m0g], 4096\n"
      "s_mov_b32 m0, s44\n"
      "s_nop 0\n"
      "global_load_lds_dwordx4 %[o4], s[40:41] offset:0\n"
      "v_mfma_f32_32x32x16_bf16 v[112:127], v[184:187], v[192:195], v[112:127]\n"
      "global_load_lds_dwordx4 %[o5], s[40:41] offset:1024\n"
      "s_waitcnt lgkmcnt(3)\n"
      "v_add_u32 %[x3], s46, %[x3]\n"
      "v_mfma_f32_32x32x16_bf16 v[64:79], v[164:167], v[172:175], v[64:79]\n"
      "ds_read_b128 v[180:183], %[x3]\n"
      "ds_read_b64_tr_b16 v[188:189], %[wa0] offset:24576\n"
      "ds_read_b64_tr_b16 v[190:191], %[wa0] offset:25600\n"
      "s_waitcnt lgkmcnt(4)\n"
      "v_mfma_f32_32x32x16_bf16 v[80:95], v[164:167], v[176:179], v[80:95]\n"
      "ds_read_b64_tr_b16 v[192:193], %[wa1] offset:24576\n"
      "ds_read_b64_tr_b16 v[194:195], %[wa1] offset:25600\n"
      "ds_read_b128 v[184:187], %[x3] offset:8192\n"
      "s_waitcnt lgkmcnt(6)\n"
      "v_mfma_f32_32x32x16_bf16 v[96:111], v[168:171], v[172:175], v[96:111]\n"
      "global_load_lds_dwordx4 %[o6], s[42:43] offset:2048\n"
      "v_mfma_f32_32x32x16_bf16 v[112:127], v[168:171], v[176:179], v[112:127]\n"
      "global_load_lds_dwordx4 %[o7], s[42:43] offset:3072\n"
      "s_add_u32 %[kt], %[kt], 1\n"
      "s_cmp_eq_u32 %[kt], 10\n"
      "s_cselect_b32 s44, 0xfffffb80, s48\n"
      "s_cselect_b32 s45, 0xffde4000, s49\n"
      "s_cselect_b32 %[kt], 0, %[kt]\n"
      "s_cselect_b32 s47, -1, 0\n"
      "s_add_u32 s40, s40, s44\n"
      "s_addc_u32 s41, s41, s47\n"
      "s_add_u32 s42, s42, s45\n"
      "s_addc_u32 s43, s43, s47\n"
      "s_waitcnt vmcnt(8) lgkmcnt(0)\n"
      "s_barrier\n"
      "s_mov_b32 s46, 0x8000\n"
      "v_add_u32 %[x0], s46, %[x0]\n"
      "v_add_u32 %[wa0], s46, %[wa0]\n"
      "v_add_u32 %[wa1], s46, %[wa1]\n"
      "v_mfma_f32_32x32x16_bf16 v[64:79], v[180:183], v[188:191], v[64:79]\n"
      "ds_read_b128 v[164:167], %[x0]\n"
      "ds_read_b64_tr_b16 v[172:173], %[wa0] offset:0\n"
      "ds_read_b64_tr_b16 v[174:175], %[wa0] offset:1024\n"
      "v_mfma_f32_32x32x16_bf16 v[80:95], v[180:183], v[192:195], v[80:95]\n"
      "ds_read_b64_tr_b16 v[176:177], %[wa1] offset:0\n"
      "ds_read_b64_tr_b16 v[178:179], %[wa1] offset:1024\n"
      "ds_read_b128 v[168:171], %[x0] offset:8192\n"
      "v_mfma_f32_32x32x16_bf16 v[96:111], v[184:187], v[188:191], v[96:111]\n"
      "s_add_u32 s44, %[m0g], 32768\n"
      "s_mov_b32 m0, s44\n"
      "s_nop 0\n"
      "global_load_lds_dwordx4 %[o0], s[40:41] offset:0\n"
      "v_mfma_f32_32x32x16_bf16 v[112:127], v[184:187], v[192:195], v[112:127]\n"
      "global_load_lds_dwordx4 %[o1], s[40:41] offset:1024\n"
      "s_waitcnt lgkmcnt(3)\n"
      "v_add_u32 %[x1], s46, %[x1]\n"
      "v_mfma_f32_32x32x16_bf16 v[64:79], v[164:167], v[172:175], v[64:79]\n"
      "ds_read_b128 v[180:183], %[x1]\n"
      "ds_read_b64_tr_b16 v[188:189], %[wa0] offset:8192\n"
      "ds_read_b64_tr_b16 v[190:191], %[wa0] offset:9216\n"
      "s_waitcnt lgkmcnt(4)\n"
      "v_mfma_f32_32x32x16_bf16 v[80:95], v[164:167], v[176:179], v[80:95]\n"
      "ds_read_b64_tr_b16 v[192:193], %[wa1] offset:8192\n"
      "ds_read_b64_tr_b16 v[194:195], %[wa1] offset:9216\n"
      "ds_read_b128 v[184:187], %[x1] offset:8192\n"
      "s_waitcnt lgkmcnt(6)\n"
      "v_mfma_f32_32x32x16_bf16 v[96:111], v[168:171], v[172:175], v[96:111]\n"
      "global_load_lds_dwordx4 %[o2], s[42:43] offset:2048\n"
      "v_mfma_f32_32x32x16_bf16 v[112:127], v[168:171], v[176:179], v[112:127]\n"
      "global_load_lds_dwordx4 %[o3], s[42:43] offset:3072\n"
      "s_waitcnt lgkmcnt(3)\n"
      "v_add_u32 %[x2], s46, %[x2]\n"
      "v_mfma_f32_32x32x16_bf16 v[64:79], v[180:183], v[188:191], v[64:79]\n"
      "ds_read_b128 v[164:167], %[x2]\n"
      "ds_read_b64_tr_b16 v[172:173], %[wa0] offset:16384\n"
      "ds_read_b64_tr_b16 v[174:175], %[wa0] offset:17408\n"
      "s_waitcnt lgkmcnt(4)\n"
      "v_mfma_f32_32x32x16_bf16 v[80:95], v[180:183], v[192:195], v[80:95]\n"
      "ds_read_b64_tr_b16 v[176:177], %[wa1] offset:16384\n"
      "ds_read_b64_tr_b16 v[178:179], %[wa1] offset:17408\n"
      "ds_read_b128 v[168:171], %[x2] offset:8192\n"
      "s_waitcnt lgkmcnt(6)\n"
      "v_mfma_f32_32x32x16_bf16 v[96:111], v[184:187], v[188:191], v[96:111]\n"
      "s_add_u32 s44, %[m0g], 36864\n"
      "s_mov_b32 m0, s44\n"
      "s_nop 0\n"
      "global_load_lds_dwordx4 %[o4], s[40:41] offset:0\n"
      "v_mfma_f32_32x32x16_bf16 v[112:127], v[184:187], v[192:195], v[112:127]\n"
      "global_load_lds_dwordx4 %[o5], s[40:41] offset:1024\n"
      "s_waitcnt lgkmcnt(3)\n"
      "v_add_u32 %[x3], s46, %[x3]\n"
      "v_mfma_f32_32x32x16_bf16 v[64:79], v[164:167], v[172:175], v[64:79]\n"
      "ds_read_b128 v[180:183], %[x3]\n"
      "ds_read_b64_tr_b16 v[188:189], %[wa0] offset:24576\n"
      "ds_read_b64_tr_b16 v[190:191], %[wa0] offset:25600\n"
      "s_waitcnt lgkmcnt(4)\n"
      "v_mfma_f32_32x32x16_bf16 v[80:95], v[164:167], v[176:179], v[80:95]\n"
      "ds_read_b64_tr_b16 v[192:193], %[wa1] offset:24576\n"
      "ds_read_b64_tr_b16 v[194:195], %[wa1] offset:25600\n"
      "ds_read_b128 v[184:187], %[x3] offset:8192\n"
      "s_waitcnt lgkmcnt(6)\n"
      "v_mfma_f32_32x32x16_bf16 v[96:111], v[168:171], v[172:175], v[96:111]\n"
      "global_load_lds_dwordx4 %[o6], s[42:43] offset:2048\n"
      "v_mfma_f32_32x32x16_bf16 v[112:127], v[168:171], v[176:179], v[112:127]\n"
      "global_load_lds_dwordx4 %[o7], s[42:43] offset:3072\n"
      "s_add_u32 %[kt], %[kt], 1\n"
      "s_cmp_eq_u32 %[kt], 10\n"
      "s_cselect_b32 s44, 0xfffffb80, s48\n"
      "s_cselect_b32 s45, 0xffde4000, s49\n"
      "s_cselect_b32 %[kt], 0, %[kt]\n"
      "s_cselect_b32 s47, -1, 0\n"
      "s_add_u32 s40, s40, s44\n"
      "s_addc_u32 s41, s41, s47\n"
      "s_add_u32 s42, s42, s45\n"
      "s_addc_u32 s43, s43, s47\n"
      "s_waitcnt vmcnt(8) lgkmcnt(0)\n"
      "s_barrier\n"
      "s_mov_b32 s46, 0xffff0000\n"
      "v_add_u32 %[x0], s46, %[x0]\n"
      "v_add_u32 %[wa0], s46, %[wa0]\n"
      "v_add_u32 %[wa1], s46, %[wa1]\n"
      "v_mfma_f32_32x32x16_bf16 v[64:79], v[180:183], v[188:191], v[64:79]\n"
      "ds_read_b128 v[164:167], %[x0]\n"
      "ds_read_b64_tr_b16 v[172:173], %[wa0] offset:0\n"
      "ds_read_b64_tr_b16 v[174:175], %[wa0] offset:1024\n"
      "v_mfma_f32_32x32x16_bf16 v[80:95], v[180:183], v[192:195], v[80:95]\n"
      "ds_read_b64_tr_b16 v[176:177], %[wa1] offset:0\n"
      "ds_read_b64_tr_b16 v[178:179], %[wa1] offset:1024\n"
      "ds_read_b128 v[168:171], %[x0] offset:8192\n"
      "v_mfma_f32_32x32x16_bf16 v[96:111], v[184:187], v[188:191], v[96:111]\n"
      "s_add_u32 s44, %[m0g], 65536\n"
      "s_mov_b32 m0, s44\n"
      "s_nop 0\n"
      "global_load_lds_dwordx4 %[o0], s[40:41] offset:0\n"
      "v_mfma_f32_32x32x16_bf16 v[112:127], v[184:187], v[192:195], v[112:127]\n"
      "global_load_lds_dwordx4 %[o1], s[40:41] offset:1024\n"
      "s_waitcnt lgkmcnt(3)\n"
      "v_add_u32 %[x1], s46, %[x1]\n"
      "v_mfma_f32_32x32x16_bf16 v[64:79], v[164:167], v[172:175], v[64:79]\n"
      "ds_read_b128 v[180:183], %[x1]\n"
      "ds_read_b64_tr_b16 v[188:189], %[wa0] offset:8192\n"
      "ds_read_b64_tr_b16 v[190:191], %[wa0] offset:9216\n"
      "s_waitcnt lgkmcnt(4)\n"
      "v_mfma_f32_32x32x16_bf16 v[80:95], v[164:167], v[176:179], v[80:95]\n"
      "ds_read_b64_tr_b16 v[192:193], %[wa1] offset:8192\n"
      "ds_read_b64_tr_b16 v[194:195], %[wa1] offset:9216\n"
      "ds_read_b128 v[184:187], %[x1] offset:8192\n"
      "s_waitcnt lgkmcnt(6)\n"
      "v_mfma_f32_32x32x16_bf16 v[96:111], v[168:171], v[172:175], v[96:111]\n"
      "global_load_lds_dwordx4 %[o2], s[42:43] offset:2048\n"
      "v_mfma_f32_32x32x16_bf16 v[112:127], v[168:171], v[176:179], v[112:127]\n"
      "global_load_lds_dwordx4 %[o3], s[42:43] offset:3072\n"
      "s_waitcnt lgkmcnt(3)\n"
      "v_add_u32 %[x2], s46, %[x2]\n"
      "v_mfma_f32_32x32x16_bf16 v[64:79], v[180:183], v[188:191], v[64:79]\n"
      "ds_read_b128 v[164:167], %[x2]\n"
      "ds_read_b64_tr_b16 v[172:173], %[wa0] offset:16384\n"
      "ds_read_b64_tr_b16 v[174:175], %[wa0] offset:17408\n"
      "s_waitcnt lgkmcnt(4)\n"
      "v_mfma_f32_32x32x16_bf16 v[80:95], v[180:183], v[192:195], v[80:95]\n"
      "ds_read_b64_tr_b16 v[176:177], %[wa1] offset:16384\n"
      "ds_read_b64_tr_b16 v[178:179], %[wa1] offset:17408\n"
      "ds_read_b128 v[168:171], %[x2] offset:8192\n"
      "s_waitcnt lgkmcnt(6)\n"
      "v_mfma_f32_32x32x16_bf16 v[96:111], v[184:187], v[188:191], v[96:111]\n"
      "s_add_u32 s44, %[m0g], 69632\n"
      "s_mov_b32 m0, s44\n"
      "s_nop 0\n"
      "global_load_lds_dwordx4 %[o4], s[40:41] offset:0\n"
      "v_mfma_f32_32x32x16_bf16 v[112:127], v[184:187], v[192:195], v[112:127]\n"
      "global_load_lds_dwordx4 %[o5], s[40:41] offset:1024\n"
      "s_waitcnt lgkmcnt(3)\n"
      "v_add_u32 %[x3], s46, %[x3]\n"
      "v_mfma_f32_32x32x16_bf16 v[64:79], v[164:167], v[172:175], v[64:79]\n"
      "ds_read_b128 v[180:183], %[x3]\n"
      "ds_read_b64_tr_b16 v[188:189], %[wa0] offset:24576\n"
      "ds_read_b64_tr_b16 v[190:191], %[wa0] offset:25600\n"
      "s_waitcnt lgkmcnt(4)\n"
      "v_mfma_f32_32x32x16_bf16 v[80:95], v[164:167], v[176:179], v[80:95]\n"
      "ds_read_b64_tr_b16 v[192:193], %[wa1] offset:24576\n"
      "ds_read_b64_tr_b16 v[194:195], %[wa1] offset:25600\n"
      "ds_read_b128 v[184:187], %[x3] offset:8192\n"
      "s_waitcnt lgkmcnt(6)\n"
      "v_mfma_f32_32x32x16_bf16 v[96:111], v[168:171], v[172:175], v[96:111]\n"
      "global_load_lds_dwordx4 %[o6], s[42:43] offset:2048\n"
      "v_mfma_f32_32x32x16_bf16 v[112:127], v[168:171], v[176:179], v[112:127]\n"
      "global_load_lds_dwordx4 %[o7], s[42:43] offset:3072\n"
      "s_add_u32 %[kt], %[kt], 1\n"
      "s_cmp_eq_u32 %[kt], 10\n"
      "s_cselect_b32 s44, 0xfffffb80, s48\n"
      "s_cselect_b32 s45, 0xffde4000, s49\n"
      "s_cselect_b32 %[kt], 0, %[kt]\n"
      "s_cselect_b32 s47, -1, 0\n"
      "s_add_u32 s40, s40, s44\n"
      "s_addc_u32 s41, s41, s47\n"
      "s_add_u32 s42, s42, s45\n"
      "s_addc_u32 s43, s43, s47\n"
      "s_waitcnt vmcnt(8) lgkmcnt(0)\n"
      "s_barrier\n"
      "s_mov_b32 s46, 0x8000\n"
      "v_add_u32 %[x0], s46, %[x0]\n"
      "v_add_u32 %[wa0], s46, %[wa0]\n"
      "v_add_u32 %[wa1], s46, %[wa1]\n"
      "v_mfma_f32_32x32x16_bf16 v[64:79], v[180:183], v[188:191], v[64:79]\n"
      "ds_read_b128 v[164:167], %[x0]\n"
      "ds_read_b64_tr_b16 v[172:173], %[wa0] offset:0\n"
      "ds_read_b64_tr_b16 v[174:175], %[wa0] offset:1024\n"
      "v_mfma_f32_32x32x16_bf16 v[80:95], v[180:183], v[192:195], v[80:95]\n"
      "ds_read_b64_tr_b16 v[176:177], %[wa1] offset:0\n"
      "ds_read_b64_tr_b16 v[178:179], %[wa1] offset:1024\n"
      "ds_read_b128 v[168:171], %[x0] offset:8192\n"
      "v_mfma_f32_32x32x16_bf16 v[96:111], v[184:187], v[188:191], v[96:111]\n"
      "s_add_u32 s44, %[m0g], 0\n"
      "s_mov_b32 m0, s44\n"
      "s_nop 0\n"
      "global_load_lds_dwordx4 %[o0], s[40:41] offset:0\n"
      "v_mfma_f32_32x32x16_bf16 v[112:127], v[184:187], v[192:195], v[112:127]\n"
      "global_load_lds_dwordx4 %[o1], s[40:41] offset:1024\n"
      "s_waitcnt lgkmcnt(3)\n"
      "v_add_u32 %[x1], s46, %[x1]\n"
      "v_mfma_f32_32x32x16_bf16 v[64:79], v[164:167], v[172:175], v[64:79]\n"
      "ds_read_b128 v[180:183], %[x1]\n"
      "ds_read_b64_tr_b16 v[188:189], %[wa0] offset:8192\n"
      "ds_read_b64_tr_b16 v[190:191], %[wa0] offset:9216\n"
      "s_waitcnt lgkmcnt(4)\n"
      "v_mfma_f32_32x32x16_bf16 v[80:95], v[164:167], v[176:179], v[80:95]\n"
      "ds_read_b64_tr_b16 v[192:193], %[wa1] offset:8192\n"
      "ds_read_b64_tr_b16 v[194:195], %[wa1] offset:9216\n"
      "ds_read_b128 v[184:187], %[x1] offset:8192\n"
      "s_waitcnt lgkmcnt(6)\n"
      "v_mfma_f32_32x32x16_bf16 v[96:111], v[168:171], v[172:175], v[96:111]\n"
      "global_load_lds_dwordx4 %[o2], s[42:43] offset:2048\n"
      "v_mfma_f32_32x32x16_bf16 v[112:127], v[168:171], v[176:179], v[112:127]\n"
      "global_load_lds_dwordx4 %[o3], s[42:43] offset:3072\n"
      "s_waitcnt lgkmcnt(3)\n"
      "v_add_u32 %[x2], s46, %[x2]\n"
      "v_mfma_f32_32x32x16_bf16 v[64:79], v[180:183], v[188:191], v[64:79]\n"
      "ds_read_b128 v[164:167], %[x2]\n"
      "ds_read_b64_tr_b16 v[172:173], %[wa0] offset:16384\n"
      "ds_read_b64_tr_b16 v[174:175], %[wa0] offset:17408\n"
      "s_waitcnt lgkmcnt(4)\n"
      "v_mfma_f32_32x32x16_bf16 v[80:95], v[180:183], v[192:195], v[80:95]\n"
      "ds_read_b64_tr_b16 v[176:177], %[wa1] offset:16384\n"
      "ds_read_b64_tr_b16 v[178:179], %[wa1] offset:17408\n"
      "ds_read_b128 v[168:171], %[x2] offset:8192\n"
      "s_waitcnt lgkmcnt(6)\n"
      "v_mfma_f32_32x32x16_bf16 v[96:111], v[184:187], v[188:191], v[96:111]\n"
      "s_add_u32 s44, %[m0g], 4096\n"
      "s_mov_b32 m0, s44\n"
      "s_nop 0\n"
      "global_load_lds_dwordx4 %[o4], s[40:41] offset:0\n"
      "v_mfma_f32_32x32x16_bf16 v[112:127], v[184:187], v[192:195], v[112:127]\n"
      "global_load_lds_dwordx4 %[o5], s[40:41] offset:1024\n"
      "s_waitcnt lgkmcnt(3)\n"
      "v_add_u32 %[x3], s46, %[x3]\n"
      "v_mfma_f32_32x32x16_bf16 v[64:79], v[164:167], v[172:175], v[64:79]\n"
      "ds_read_b128 v[180:183], %[x3]\n"
      "ds_read_b64_tr_b16 v[188:189], %[wa0] offset:24576\n"
      "ds_read_b64_tr_b16 v[190:191], %[wa0] offset:25600\n"
      "s_waitcnt lgkmcnt(4)\n"
      "v_mfma_f32_32x32x16_bf16 v[80:95], v[164:167], v[176:179], v[80:95]\n"
      "ds_read_b64_tr_b16 v[192:193], %[wa1] offset:24576\n"
      "ds_read_b64_tr_b16 v[194:195], %[wa1] offset:25600\n"
      "ds_read_b128 v[184:187], %[x3] offset:8192\n"
      "s_waitcnt lgkmcnt(6)\n"
      "v_mfma_f32_32x32x16_bf16 v[96:111], v[168:171], v[172:175], v[96:111]\n"
      "global_load_lds_dwordx4 %[o6], s[42:43] offset:2048\n"
      "v_mfma_f32_32x32x16_bf16 v[112:127], v[168:171], v[176:179], v[112:127]\n"
      "global_load_lds_dwordx4 %[o7], s[42:43] offset:3072\n"
      "s_add_u32 %[kt], %[kt], 1\n"
      "s_cmp_eq_u32 %[kt], 10\n"
      "s_cselect_b32 s44, 0xfffffb80, s48\n"
      "s_cselect_b32 s45, 0xffde4000, s49\n"
      "s_cselect_b32 %[kt], 0, %[kt]\n"
      "s_cselect_b32 s47, -1, 0\n"
      "s_add_u32 s40, s40, s44\n"
      "s_addc_u32 s41, s41, s47\n"
      "s_add_u32 s42, s42, s45\n"
      "s_addc_u32 s43, s43, s47\n"
      "s_waitcnt vmcnt(8) lgkmcnt(0)\n"
      "s_barrier\n"
      "s_mov_b32 s46, 0x8000\n"
      "v_add_u32 %[x0], s46, %[x0]\n"
      "v_add_u32 %[wa0], s46, %[wa0]\n"
      "v_add_u32 %[wa1], s46, %[wa1]\n"
      "v_mfma_f32_32x32x16_bf16 v[64:79], v[180:183], v[188:191], v[64:79]\n"
      "ds_read_b128 v[164:167], %[x0]\n"
      "ds_read_b64_tr_b16 v[172:173], %[wa0] offset:0\n"
      "ds_read_b64_tr_b16 v[174:175], %[wa0] offset:1024\n"
      "v_mfma_f32_32x32x16_bf16 v[80:95], v[180:183], v[192:195], v[80:95]\n"
      "ds_read_b64_tr_b16 v[176:177], %[wa1] offset:0\n"
      "ds_read_b64_tr_b16 v[178:179], %[wa1] offset:1024\n"
      "ds_read_b128 v[168:171], %[x0] offset:8192\n"
      "v_mfma_f32_32x32x16_bf16 v[96:111], v[184:187], v[188:191], v[96:111]\n"
      "s_add_u32 s44, %[m0g], 32768\n"
      "s_mov_b32 m0, s44\n"
      "s_nop 0\n"
      "global_load_lds_dwordx4 %[o0], s[40:41] offset:0\n"
      "v_mfma_f32_32x32x16_bf16 v[112:127], v[184:187], v[192:195], v[112:127]\n"
      "global_load_lds_dwordx4 %[o1], s[40:41] offset:1024\n"
      "s_waitcnt lgkmcnt(3)\n"
      "v_add_u32 %[x1], s46, %[x1]\n"
      "v_mfma_f32_32x32x16_bf16 v[64:79], v[164:167], v[172:175], v[64:79]\n"
      "ds_read_b128 v[180:183], %[x1]\n"
      "ds_read_b64_tr_b16 v[188:189], %[wa0] offset:8192\n"
      "ds_read_b64_tr_b16 v[190:191], %[wa0] offset:9216\n"
      "s_waitcnt lgkmcnt(4)\n"
      "v_mfma_f32_32x32x16_bf16 v[80:95], v[164:167], v[176:179], v[80:95]\n"
      "ds_read_b64_tr_b16 v[192:193], %[wa1] offset:8192\n"
      "ds_read_b64_tr_b16 v[194:195], %[wa1] offset:9216\n"
      "ds_read_b128 v[184:187], %[x1] offset:8192\n"
      "s_waitcnt lgkmcnt(6)\n"
      "v_mfma_f32_32x32x16_bf16 v[96:111], v[168:171], v[172:175], v[96:111]\n"
      "global_load_lds_dwordx4 %[o2], s[42:43] offset:2048\n"
      "v_mfma_f32_32x32x16_bf16 v[112:127], v[168:171], v[176:179], v[112:127]\n"
      "global_load_lds_dwordx4 %[o3], s[42:43] offset:3072\n"
      "s_waitcnt lgkmcnt(3)\n"
      "v_add_u32 %[x2], s46, %[x2]\n"
      "v_mfma_f32_32x32x16_bf16 v[64:79], v[180:183], v[188:191], v[64:79]\n"
      "ds_read_b128 v[164:167], %[x2]\n"
      "ds_read_b64_tr_b16 v[172:173], %[wa0] offset:16384\n"
      "ds_read_b64_tr_b16 v[174:175], %[wa0] offset:17408\n"
      "s_waitcnt lgkmcnt(4)\n"
      "v_mfma_f32_32x32x16_bf16 v[80:95], v[180:183], v[192:195], v[80:95]\n"
      "ds_read_b64_tr_b16 v[176:177], %[wa1] offset:16384\n"
      "ds_read_b64_tr_b16 v[178:179], %[wa1] offset:17408\n"
      "ds_read_b128 v[168:171], %[x2] offset:8192\n"
      "s_waitcnt lgkmcnt(6)\n"
      "v_mfma_f32_32x32x16_bf16 v[96:111], v[184:187], v[188:191], v[96:111]\n"
      "s_add_u32 s44, %[m0g], 36864\n"
      "s_mov_b32 m0, s44\n"
      "s_nop 0\n"
      "global_load_lds_dwordx4 %[o4], s[40:41] offset:0\n"
      "v_mfma_f32_32x32x16_bf16 v[112:127], v[184:187], v[192:195], v[112:127]\n"
      "global_load_lds_dwordx4 %[o5], s[40:41] offset:1024\n"
      "s_waitcnt lgkmcnt(3)\n"
      "v_add_u32 %[x3], s46, %[x3]\n"
      "v_mfma_f32_32x32x16_bf16 v[64:79], v[164:167], v[172:175], v[64:79]\n"
      "ds_read_b128 v[180:183], %[x3]\n"
      "ds_read_b64_tr_b16 v[188:189], %[wa0] offset:24576\n"
      "ds_read_b64_tr_b16 v[190:191], %[wa0] offset:25600\n"
      "s_waitcnt lgkmcnt(4)\n"
      "v_mfma_f32_32x32x16_bf16 v[80:95], v[164:167], v[176:179], v[80:95]\n"
      "ds_read_b64_tr_b16 v[192:193], %[wa1] offset:24576\n"
      "ds_read_b64_tr_b16 v[194:195], %[wa1] offset:25600\n"
      "ds_read_b128 v[184:187], %[x3] offset:8192\n"
      "s_waitcnt lgkmcnt(6)\n"
      "v_mfma_f32_32x32x16_bf16 v[96:111], v[168:171], v[172:175], v[96:111]\n"
      "global_load_lds_dwordx4 %[o6], s[42:43] offset:2048\n"
      "v_mfma_f32_32x32x16_bf16 v[112:127], v[168:171], v[176:179], v[112:127]\n"
      "global_load_lds_dwordx4 %[o7], s[42:43] offset:3072\n"
      "s_add_u32 %[kt], %[kt], 1\n"
      "s_cmp_eq_u32 %[kt], 10\n"
      "s_cselect_b32 s44, 0xfffffb80, s48\n"
      "s_cselect_b32 s45, 0xffde4000, s49\n"
      "s_cselect_b32 %[kt], 0, %[kt]\n"
      "s_cselect_b32 s47, -1, 0\n"
      "s_add_u32 s40, s40, s44\n"
      "s_addc_u32 s41, s41, s47\n"
      "s_add_u32 s42, s42, s45\n"
      "s_addc_u32 s43, s43, s47\n"
      "s_waitcnt vmcnt(8) lgkmcnt(0)\n"
      "s_barrier\n"
      "s_mov_b32 s46, 0xffff0000\n"
      "v_add_u32 %[x0], s46, %[x0]\n"
      "v_add_u32 %[wa0], s46, %[wa0]\n"
      "v_add_u32 %[wa1], s46, %[wa1]\n"
      "v_mfma_f32_32x32x16_bf16 v[64:79], v[180:183], v[188:191], v[64:79]\n"
      "ds_read_b128 v[164:167], %[x0]\n"
      "ds_read_b64_tr_b16 v[172:173], %[wa0] offset:0\n"
      "ds_read_b64_tr_b16 v[174:175], %[wa0] offset:1024\n"
      "v_mfma_f32_32x32x16_bf16 v[80:95], v[180:183], v[192:195], v[80:95]\n"
      "ds_read_b64_tr_b16 v[176:177], %[wa1] offset:0\n"
      "ds_read_b64_tr_b16 v[178:179], %[wa1] offset:1024\n"
      "ds_read_b128 v[168:171], %[x0] offset:8192\n"
      "v_mfma_f32_32x32x16_bf16 v[96:111], v[184:187], v[188:191], v[96:111]\n"
      "s_add_u32 s44, %[m0g], 65536\n"
      "s_mov_b32 m0, s44\n"
      "s_nop 0\n"
      "global_load_lds_dwordx4 %[o0], s[40:41] offset:0\n"
      "v_mfma_f32_32x32x16_bf16 v[112:127], v[184:187], v[192:195], v[112:127]\n"
      "global_load_lds_dwordx4 %[o1], s[40:41] offset:1024\n"
      "s_waitcnt lgkmcnt(3)\n"
      "v_add_u32 %[x1], s46, %[x1]\n"
      "v_mfma_f32_32x32x16_bf16 v[64:79], v[164:167], v[172:175], v[64:79]\n"
      "ds_read_b128 v[180:183], %[x1]\n"
      "ds_read_b64_tr_b16 v[188:189], %[wa0] offset:8192\n"
      "ds_read_b64_tr_b16 v[190:191], %[wa0] offset:9216\n"
      "s_waitcnt lgkmcnt(4)\n"
      "v_mfma_f32_32x32x16_bf16 v[80:95], v[164:167], v[176:179], v[80:95]\n"
      "ds_read_b64_tr_b16 v[192:193], %[wa1] offset:8192\n"
      "ds_read_b64_tr_b16 v[194:195], %[wa1] offset:9216\n"
      "ds_read_b128 v[184:187], %[x1] offset:8192\n"
      "s_waitcnt lgkmcnt(6)\n"
      "v_mfma_f32_32x32x16_bf16 v[96:111], v[168:171], v[172:175], v[96:111]\n"
      "global_load_lds_dwordx4 %[o2], s[42:43] offset:2048\n"
      "v_mfma_f32_32x32x16_bf16 v[112:127], v[168:171], v[176:179], v[112:127]\n"
      "global_load_lds_dwordx4 %[o3], s[42:43] offset:3072\n"
      "s_waitcnt lgkmcnt(3)\n"
      "v_add_u32 %[x2], s46, %[x2]\n"
      "v_mfma_f32_32x32x16_bf16 v[64:79], v[180:183], v[188:191], v[64:79]\n"
      "ds_read_b128 v[164:167], %[x2]\n"
      "ds_read_b64_tr_b16 v[172:173], %[wa0] offset:16384\n"
      "ds_read_b64_tr_b16 v[174:175], %[wa0] offset:17408\n"
      "s_waitcnt lgkmcnt(4)\n"
      "v_mfma_f32_32x32x16_bf16 v[80:95], v[180:183], v[192:195], v[80:95]\n"
      "ds_read_b64_tr_b16 v[176:177], %[wa1] offset:16384\n"
      "ds_read_b64_tr_b16 v[178:179], %[wa1] offset:17408\n"
      "ds_read_b128 v[168:171], %[x2] offset:8192\n"
      "s_waitcnt lgkmcnt(6)\n"
      "v_mfma_f32_32x32x16_bf16 v[96:111], v[184:187], v[188:191], v[96:111]\n"
      "s_add_u32 s44, %[m0g], 69632\n"
      "s_mov_b32 m0, s44\n"
      "s_nop 0\n"
      "global_load_lds_dwordx4 %[o4], s[40:41] offset:0\n"
      "v_mfma_f32_32x32x16_bf16 v[112:127], v[184:187], v[192:195], v[112:127]\n"
      "global_load_lds_dwordx4 %[o5], s[40:41] offset:1024\n"
      "s_waitcnt lgkmcnt(3)\n"
      "v_add_u32 %[x3], s46, %[x3]\n"
      "v_mfma_f32_32x32x16_bf16 v[64:79], v[164:167], v[172:175], v[64:79]\n"
      "ds_read_b128 v[180:183], %[x3]\n"
      "ds_read_b64_tr_b16 v[188:189], %[wa0] offset:24576\n"
      "ds_read_b64_tr_b16 v[190:191], %[wa0] offset:25600\n"
      "s_waitcnt lgkmcnt(4)\n"
      "v_mfma_f32_32x32x16_bf16 v[80:95], v[164:167], v[176:179], v[80:95]\n"
      "ds_read_b64_tr_b16 v[192:193], %[wa1] offset:24576\n"
      "ds_read_b64_tr_b16 v[194:195], %[wa1] offset:25600\n"
      "ds_read_b128 v[184:187], %[x3] offset:8192\n"
      "s_waitcnt lgkmcnt(6)\n"
      "v_mfma_f32_32x32x16_bf16 v[96:111], v[168:171], v[172:175], v[96:111]\n"
      "global_load_lds_dwordx4 %[o6], s[42:43] offset:2048\n"
      "v_mfma_f32_32x32x16_bf16 v[112:127], v[168:171], v[176:179], v[112:127]\n"
      "global_load_lds_dwordx4 %[o7], s[42:43] offset:3072\n"
      "s_add_u32 %[kt], %[kt], 1\n"
      "s_cmp_eq_u32 %[kt], 10\n"
      "s_cselect_b32 s44, 0xfffffb80, s48\n"
      "s_cselect_b32 s45, 0xffde4000, s49\n"
      "s_cselect_b32 %[kt], 0, %[kt]\n"
      "s_cselect_b32 s47, -1, 0\n"
      "s_add_u32 s40, s40, s44\n"
      "s_addc_u32 s41, s41, s47\n"
      "s_add_u32 s42, s42, s45\n"
      "s_addc_u32 s43, s43, s47\n"
      "s_waitcnt vmcnt(8) lgkmcnt(0)\n"
      "s_barrier\n"
      "s_mov_b32 s46, 0x8000\n"
      "v_add_u32 %[x0], s46, %[x0]\n"
      "v_add_u32 %[wa0], s46, %[wa0]\n"
      "v_add_u32 %[wa1], s46, %[wa1]\n"
      "v_mfma_f32_32x32x16_bf16 v[64:79], v[180:183], v[188:191], v[64:79]\n"
      "ds_read_b128 v[164:167], %[x0]\n"
      "ds_read_b64_tr_b16 v[172:173], %[wa0] offset:0\n"
      "ds_read_b64_tr_b16 v[174:175], %[wa0] offset:1024\n"
      "v_mfma_f32_32x32x16_bf16 v[80:95], v[180:183], v[192:195], v[80:95]\n"
      "ds_read_b64_tr_b16 v[176:177], %[wa1] offset:0\n"
      "ds_read_b64_tr_b16 v[178:179], %[wa1] offset:1024\n"
      "ds_read_b128 v[168:171], %[x0] offset:8192\n"
      "v_mfma_f32_32x32x16_bf16 v[96:111], v[184:187], v[188:191], v[96:111]\n"
      "s_add_u32 s44, %[m0g], 0\n"
      "s_mov_b32 m0, s44\n"
      "s_nop 0\n"
      "global_load_lds_dwordx4 %[o0], s[40:41] offset:0\n"
      "v_mfma_f32_32x32x16_bf16 v[112:127], v[184:187], v[192:195], v[112:127]\n"
      "global_load_lds_dwordx4 %[o1], s[40:41] offset:1024\n"
      "s_waitcnt lgkmcnt(3)\n"
      "v_add_u32 %[x1], s46, %[x1]\n"
      "v_mfma_f32_32x32x16_bf16 v[64:79], v[164:167], v[172:175], v[64:79]\n"
      "ds_read_b128 v[180:183], %[x1]\n"
      "ds_read_b64_tr_b16 v[188:189], %[wa0] offset:8192\n"
      "ds_read_b64_tr_b16 v[190:191], %[wa0] offset:9216\n"
      "s_waitcnt lgkmcnt(4)\n"
      "v_mfma_f32_32x32x16_bf16 v[80:95], v[164:167], v[176:179], v[80:95]\n"
      "ds_read_b64_tr_b16 v[192:193], %[wa1] offset:8192\n"
      "ds_read_b64_tr_b16 v[194:195], %[wa1] offset:9216\n"
      "ds_read_b128 v[184:187], %[x1] offset:8192\n"
      "s_waitcnt lgkmcnt(6)\n"
      "v_mfma_f32_32x32x16_bf16 v[96:111], v[168:171], v[172:175], v[96:111]\n"
      "global_load_lds_dwordx4 %[o2], s[42:43] offset:2048\n"
      "v_mfma_f32_32x32x16_bf16 v[112:127], v[168:171], v[176:179], v[112:127]\n"
      "global_load_lds_dwordx4 %[o3], s[42:43] offset:3072\n"
      "s_waitcnt lgkmcnt(3)\n"
      "v_add_u32 %[x2], s46, %[x2]\n"
      "v_mfma_f32_32x32x16_bf16 v[64:79], v[180:183], v[188:191], v[64:79]\n"
      "ds_read_b128 v[164:167], %[x2]\n"
      "ds_read_b64_tr_b16 v[172:173], %[wa0] offset:16384\n"
      "ds_read_b64_tr_b16 v[174:175], %[wa0] offset:17408\n"
      "s_waitcnt lgkmcnt(4)\n"
      "v_mfma_f32_32x32x16_bf16 v[80:95], v[180:183], v[192:195], v[80:95]\n"
      "ds_read_b64_tr_b16 v[176:177], %[wa1] offset:16384\n"
      "ds_read_b64_tr_b16 v[178:179], %[wa1] offset:17408\n"
      "ds_read_b128 v[168:171], %[x2] offset:8192\n"
      "s_waitcnt lgkmcnt(6)\n"
      "v_mfma_f32_32x32x16_bf16 v[96:111], v[184:187], v[188:191], v[96:111]\n"
      "s_add_u32 s44, %[m0g], 4096\n"
      "s_mov_b32 m0, s44\n"
      "s_nop 0\n"
      "global_load_lds_dwordx4 %[o4], s[40:41] offset:0\n"
      "v_mfma_f32_32x32x16_bf16 v[112:127], v[184:187], v[192:195], v[112:127]\n"
      "global_load_lds_dwordx4 %[o5], s[40:41] offset:1024\n"
      "s_waitcnt lgkmcnt(3)\n"
      "v_add_u32 %[x3], s46, %[x3]\n"
      "v_mfma_f32_32x32x16_bf16 v[64:79], v[164:167], v[172:175], v[64:79]\n"
      "ds_read_b128 v[180:183], %[x3]\n"
      "ds_read_b64_tr_b16 v[188:189], %[wa0] offset:24576\n"
      "ds_read_b64_tr_b16 v[190:191], %[wa0] offset:25600\n"
      "s_waitcnt lgkmcnt(4)\n"
      "v_mfma_f32_32x32x16_bf16 v[80:95], v[164:167], v[176:179], v[80:95]\n"
      "ds_read_b64_tr_b16 v[192:193], %[wa1] offset:24576\n"
      "ds_read_b64_tr_b16 v[194:195], %[wa1] offset:25600\n"
      "ds_read_b128 v[184:187], %[x3] offset:8192\n"
      "s_waitcnt lgkmcnt(6)\n"
      "v_mfma_f32_32x32x16_bf16 v[96:111], v[168:171], v[172:175], v[96:111]\n"
      "global_load_lds_dwordx4 %[o6], s[42:43] offset:2048\n"
      "v_mfma_f32_32x32x16_bf16 v[112:127], v[168:171], v[176:179], v[112:127]\n"
      "global_load_lds_dwordx4 %[o7], s[42:43] offset:3072\n"
      "s_waitcnt vmcnt(8) lgkmcnt(0)\n"
      "s_barrier\n"
      "s_mov_b32 s46, 0x8000\n"
      "v_add_u32 %[x0], s46, %[x0]\n"
      "v_add_u32 %[wa0], s46, %[wa0]\n"
      "v_add_u32 %[wa1], s46, %[wa1]\n"
      "v_mfma_f32_32x32x16_bf16 v[64:79], v[180:183], v[188:191], v[64:79]\n"
      "ds_read_b128 v[164:167], %[x0]\n"
      "ds_read_b64_tr_b16 v[172:173], %[wa0] offset:0\n"
      "ds_read_b64_tr_b16 v[174:175], %[wa0] offset:1024\n"
      "v_mfma_f32_32x32x16_bf16 v[80:95], v[180:183], v[192:195], v[80:95]\n"
      "ds_read_b64_tr_b16 v[176:177], %[wa1] offset:0\n"
      "ds_read_b64_tr_b16 v[178:179], %[wa1] offset:1024\n"
      "ds_read_b128 v[168:171], %[x0] offset:8192\n"
      "v_mfma_f32_32x32x16_bf16 v[96:111], v[184:187], v[188:191], v[96:111]\n"
      "v_mfma_f32_32x32x16_bf16 v[112:127], v[184:187], v[192:195], v[112:127]\n"
      "s_waitcnt lgkmcnt(3)\n"
      "v_add_u32 %[x1], s46, %[x1]\n"
      "v_mfma_f32_32x32x16_bf16 v[64:79], v[164:167], v[172:175], v[64:79]\n"
      "ds_read_b128 v[180:183], %[x1]\n"
      "ds_read_b64_tr_b16 v[188:189], %[wa0] offset:8192\n"
      "ds_read_b64_tr_b16 v[190:191], %[wa0] offset:9216\n"
      "s_waitcnt lgkmcnt(4)\n"
      "v_mfma_f32_32x32x16_bf16 v[80:95], v[164:167], v[176:179], v[80:95]\n"
      "ds_read_b64_tr_b16 v[192:193], %[wa1] offset:8192\n"
      "ds_read_b64_tr_b16 v[194:195], %[wa1] offset:9216\n"
      "ds_read_b128 v[184:187], %[x1] offset:8192\n"
      "s_waitcnt lgkmcnt(6)\n"
      "v_mfma_f32_32x32x16_bf16 v[96:111], v[168:171], v[172:175], v[96:111]\n"
      "v_mfma_f32_32x32x16_bf16 v[112:127], v[168:171], v[176:179], v[112:127]\n"
      "s_waitcnt lgkmcnt(3)\n"
      "v_add_u32 %[x2], s46, %[x2]\n"
      "v_mfma_f32_32x32x16_bf16 v[64:79], v[180:183], v[188:191], v[64:79]\n"
      "ds_read_b128 v[164:167], %[x2]\n"
      "ds_read_b64_tr_b16 v[172:173], %[wa0] offset:16384\n"
      "ds_read_b64_tr_b16 v[174:175], %[wa0] offset:17408\n"
      "s_waitcnt lgkmcnt(4)\n"
      "v_mfma_f32_32x32x16_bf16 v[80:95], v[180:183], v[192:195], v[80:95]\n"
      "ds_read_b64_tr_b16 v[176:177], %[wa1] offset:16384\n"
      "ds_read_b64_tr_b16 v[178:179], %[wa1] offset:17408\n"
      "ds_read_b128 v[168:171], %[x2] offset:8192\n"
      "s_waitcnt lgkmcnt(6)\n"
      "v_mfma_f32_32x32x16_bf16 v[96:111], v[184:187], v[188:191], v[96:111]\n"
      "v_mfma_f32_32x32x16_bf16 v[112:127], v[184:187], v[192:195], v[112:127]\n"
      "s_waitcnt lgkmcnt(3)\n"
      "v_add_u32 %[x3], s46, %[x3]\n"
      "v_mfma_f32_32x32x16_bf16 v[64:79], v[164:167], v[172:175], v[64:79]\n"
      "ds_read_b128 v[180:183], %[x3]\n"
      "ds_read_b64_tr_b16 v[188:189], %[wa0] offset:24576\n"
      "ds_read_b64_tr_b16 v[190:191], %[wa0] offset:25600\n"
      "s_waitcnt lgkmcnt(4)\n"
      "v_mfma_f32_32x32x16_bf16 v[80:95], v[164:167], v[176:179], v[80:95]\n"
      "ds_read_b64_tr_b16 v[192:193], %[wa1] offset:24576\n"
      "ds_read_b64_tr_b16 v[194:195], %[wa1] offset:25600\n"
      "ds_read_b128 v[184:187], %[x3] offset:8192\n"
      "s_waitcnt lgkmcnt(6)\n"
      "v_mfma_f32_32x32x16_bf16 v[96:111], v[168:171], v[172:175], v[96:111]\n"
      "v_mfma_f32_32x32x16_bf16 v[112:127], v[168:171], v[176:179], v[112:127]\n"
      "s_waitcnt vmcnt(0) lgkmcnt(0)\n"
      "s_barrier\n"
      "s_mov_b32 s46, 0xffff0000\n"
      "v_add_u32 %[x0], s46, %[x0]\n"
      "v_add_u32 %[wa0], s46, %[wa0]\n"
      "v_add_u32 %[wa1], s46, %[wa1]\n"
      "v_mfma_f32_32x32x16_bf16 v[64:79], v[180:183], v[188:191], v[64:79]\n"
      "ds_read_b128 v[164:167], %[x0]\n"
      "ds_read_b64_tr_b16 v[172:173], %[wa0] offset:0\n"
      "ds_read_b64_tr_b16 v[174:175], %[wa0] offset:1024\n"
      "v_mfma_f32_32x32x16_bf16 v[80:95], v[180:183], v[192:195], v[80:95]\n"
      "ds_read_b64_tr_b16 v[176:177], %[wa1] offset:0\n"
      "ds_read_b64_tr_b16 v[178:179], %[wa1] offset:1024\n"
      "ds_read_b128 v[168:171], %[x0] offset:8192\n"
      "v_mfma_f32_32x32x16_bf16 v[96:111], v[184:187], v[188:191], v[96:111]\n"
      "v_mfma_f32_32x32x16_bf16 v[112:127], v[184:187], v[192:195], v[112:127]\n"
      "s_waitcnt lgkmcnt(3)\n"
      "v_add_u32 %[x1], s46, %[x1]\n"
      "v_mfma_f32_32x32x16_bf16 v[64:79], v[164:167], v[172:175], v[64:79]\n"
      "ds_read_b128 v[180:183], %[x1]\n"
      "ds_read_b64_tr_b16 v[188:189], %[wa0] offset:8192\n"
      "ds_read_b64_tr_b16 v[190:191], %[wa0] offset:9216\n"
      "s_waitcnt lgkmcnt(4)\n"
      "v_mfma_f32_32x32x16_bf16 v[80:95], v[164:167], v[176:179], v[80:95]\n"
      "ds_read_b64_tr_b16 v[192:193], %[wa1] offset:8192\n"
      "ds_read_b64_tr_b16 v[194:195], %[wa1] offset:9216\n"
      "ds_read_b128 v[184:187], %[x1] offset:8192\n"
      "s_waitcnt lgkmcnt(6)\n"
      "v_mfma_f32_32x32x16_bf16 v[96:111], v[168:171], v[172:175], v[96:111]\n"
      "v_mfma_f32_32x32x16_bf16 v[112:127], v[168:171], v[176:179], v[112:127]\n"
      "s_waitcnt lgkmcnt(3)\n"
      "v_add_u32 %[x2], s46, %[x2]\n"
      "v_mfma_f32_32x32x16_bf16 v[64:79], v[180:183], v[188:191], v[64:79]\n"
      "ds_read_b128 v[164:167], %[x2]\n"
      "ds_read_b64_tr_b16 v[172:173], %[wa0] offset:16384\n"
      "ds_read_b64_tr_b16 v[174:175], %[wa0] offset:17408\n"
      "s_waitcnt lgkmcnt(4)\n"
      "v_mfma_f32_32x32x16_bf16 v[80:95], v[180:183], v[192:195], v[80:95]\n"
      "ds_read_b64_tr_b16 v[176:177], %[wa1] offset:16384\n"
      "ds_read_b64_tr_b16 v[178:179], %[wa1] offset:17408\n"
      "ds_read_b128 v[168:171], %[x2] offset:8192\n"
      "s_waitcnt lgkmcnt(6)\n"
      "v_mfma_f32_32x32x16_bf16 v[96:111], v[184:187], v[188:191], v[96:111]\n"
      "v_mfma_f32_32x32x16_bf16 v[112:127], v[184:187], v[192:195], v[112:127]\n"
      "s_waitcnt lgkmcnt(3)\n"
      "v_add_u32 %[x3], s46, %[x3]\n"
      "v_mfma_f32_32x32x16_bf16 v[64:79], v[164:167], v[172:175], v[64:79]\n"
      "ds_read_b128 v[180:183], %[x3]\n"
      "ds_read_b64_tr_b16 v[188:189], %[wa0] offset:24576\n"
      "ds_read_b64_tr_b16 v[190:191], %[wa0] offset:25600\n"
      "s_waitcnt lgkmcnt(4)\n"
      "v_mfma_f32_32x32x16_bf16 v[80:95], v[164:167], v[176:179], v[80:95]\n"
      "ds_read_b64_tr_b16 v[192:193], %[wa1] offset:24576\n"
      "ds_read_b64_tr_b16 v[194:195], %[wa1] offset:25600\n"
      "ds_read_b128 v[184:187], %[x3] offset:8192\n"
      "s_waitcnt lgkmcnt(6)\n"
      "v_mfma_f32_32x32x16_bf16 v[96:111], v[168:171], v[172:175], v[96:111]\n"
      "v_mfma_f32_32x32x16_bf16 v[112:127], v[168:171], v[176:179], v[112:127]\n"
      "s_waitcnt lgkmcnt(0)\n"
      "v_mfma_f32_32x32x16_bf16 v[64:79], v[180:183], v[188:191], v[64:79]\n"
      "v_mfma_f32_32x32x16_bf16 v[80:95], v[180:183], v[192:195], v[80:95]\n"
      "v_mfma_f32_32x32x16_bf16 v[96:111], v[184:187], v[188:191], v[96:111]\n"
      "v_mfma_f32_32x32x16_bf16 v[112:127], v[184:187], v[192:195], v[112:127]\n"
      "s_nop 15\n"
        QKV_OPS);
#pragma unroll
        for (int b = 0; b < 2; ++b) {
          st16(ws, da[b], pk8(b ? c01 : c00, 0));
          st16(ws, da[b] + 2 * V_PLANE, pk8(b ? c01 : c00, 8));
          st16(ws, da[b] + 4 * V_PLANE, pk8(b ? c11 : c10, 0));
          st16(ws, da[b] + 6 * V_PLANE, pk8(b ? c11 : c10, 8));
        }
      } else {
        const unsigned hs = isq ? Q_HEAD : KV_HEAD, base = isq ? WS_Q + tile0 * Q_TILE : WS_KV + (tile0 >> 1) * KV_PAIR;
        const unsigned bo = (isq ? 1024 : 2048) + r8, ta = isq ? Q_TILE : 1024, tbb = isq ? Q_TILE : 256;
        bool rem[2];
#pragma unroll
        for (int b = 0; b < 2; ++b) {
          const u64 qe = b ? qe1 : qe0;
          const unsigned lq = ((unsigned)(qe >> (16 * (2 * blk + (pp & 1)))) >> (5 * (pp >> 1))) & 31;
          wa[b] = h * 4096 + 2048 + q * 256 + (((lq >> 3) ^ q) << 6) + (lq & 7) * 8;
          const unsigned e0 = (unsigned)(qe >> (16 * h)) & 0xffff, e1 = (unsigned)(qe >> (32 + 16 * h)) & 0xffff;
          rem[b] = h && !((qe >> 58) & 1);
          da[b] = base + ((g5 * 32 + (e0 & 31)) / 5) * hs + ((e0 >> 11) & 1) * 512 + r * 16;
          db[b] = base + ((g5 * 32 + (e1 & 31)) / 5) * hs + (rem[b] ? bo : ((e1 >> 11) & 1) * 512 + r * 16);
          dc[b] = base + ((g5 * 32 + ((e1 >> 5) & 31)) / 5) * hs + bo;
          asm volatile("" : "+v"(da[b]), "+v"(db[b]), "+v"(dc[b]));
        }
        PRE_GEMM();
        asm volatile(
      "s_mov_b32 s48, 128\n"
      "s_mov_b32 s49, 245760\n"
      "s_cmp_eq_u32 %[m0g], 24576\n"
      "s_cbranch_scc1 1f\n"
      "s_waitcnt vmcnt(0)\n"
      "1:\n"
      "s_waitcnt vmcnt(34)\n"
      "s_barrier\n"
      "ds_read_b128 v[164:167], %[x0]\n"
      "ds_read_b64_tr_b16 v[172:173], %[wa0] offset:0\n"
      "ds_read_b64_tr_b16 v[174:175], %[wa0] offset:1024\n"
      "ds_read_b64_tr_b16 v[176:177], %[wa1] offset:0\n"
      "ds_read_b64_tr_b16 v[178:179], %[wa1] offset:1024\n"
      "ds_read_b128 v[168:171], %[x0] offset:8192\n"
      "s_waitcnt lgkmcnt(3)\n"
      "v_mfma_f32_32x32x16_bf16 v[64:79], v[172:175], v[164:167], 0\n"
      "ds_read_b128 v[180:183], %[x1]\n"
      "ds_read_b64_tr_b16 v[188:189], %[wa0] offset:8192\n"
      "ds_read_b64_tr_b16 v[190:191], %[wa0] offset:9216\n"
      "s_waitcnt lgkmcnt(4)\n"
      "v_mfma_f32_32x32x16_bf16 v[80:95], v[176:179], v[164:167], 0\n"
      "ds_read_b64_tr_b16 v[192:193], %[wa1] offset:8192\n"
      "ds_read_b64_tr_b16 v[194:195], %[wa1] offset:9216\n"
      "ds_read_b128 v[184:187], %[x1] offset:8192\n"
      "s_waitcnt lgkmcnt(6)\n"
      "v_mfma_f32_32x32x16_bf16 v[96:111], v[172:175], v[168:171], 0\n"
      "v_mfma_f32_32x32x16_bf16 v[112:127], v[176:179], v[168:171], 0\n"
      "s_waitcnt lgkmcnt(3)\n"
      "v_mfma_f32_32x32x16_bf16 v[64:79], v[188:191], v[180:183], v[64:79]\n"
      "ds_read_b128 v[164:167], %[x2]\n"
      "ds_read_b64_tr_b16 v[172:173], %[wa0] offset:16384\n"
      "ds_read_b64_tr_b16 v[174:175], %[wa0] offset:17408\n"
      "s_waitcnt lgkmcnt(4)\n"
      "v_mfma_f32_32x32x16_bf16 v[80:95], v[192:195], v[180:183], v[80:95]\n"
      "ds_read_b64_tr_b16 v[176:177], %[wa1] offset:16384\n"
      "ds_read_b64_tr_b16 v[178:179], %[wa1] offset:17408\n"
      "ds_read_b128 v[168:171], %[x2] offset:8192\n"
      "s_waitcnt lgkmcnt(6)\n"
      "v_mfma_f32_32x32x16_bf16 v[96:111], v[188:191], v[184:187], v[96:111]\n"
      "v_mfma_f32_32x32x16_bf16 v[112:127], v[192:195], v[184:187], v[112:127]\n"
      "s_waitcnt lgkmcnt(3)\n"
      "v_mfma_f32_32x32x16_bf16 v[64:79], v[172:175], v[164:167], v[64:79]\n"
      "ds_read_b128 v[180:183], %[x3]\n"
      "ds_read_b64_tr_b16 v[188:189], %[wa0] offset:24576\n"
      "ds_read_b64_tr_b16 v[190:191], %[wa0] offset:25600\n"
      "s_waitcnt lgkmcnt(4)\n"
      "v_mfma_f32_32x32x16_bf16 v[80:95], v[176:179], v[164:167], v[80:95]\n"
      "ds_read_b64_tr_b16 v[192:193], %[wa1] offset:24576\n"
      "ds_read_b64_tr_b16 v[194:195], %[wa1] offset:25600\n"
      "ds_read_b128 v[184:187], %[x3] offset:8192\n"
      "s_waitcnt lgkmcnt(6)\n"
      "v_mfma_f32_32x32x16_bf16 v[96:111], v[172:175], v[168:171], v[96:111]\n"
      "v_mfma_f32_32x32x16_bf16 v[112:127], v[176:179], v[168:171], v[112:127]\n"
      "s_waitcnt vmcnt(0) lgkmcnt(0)\n"
      "s_barrier\n"
      "s_mov_b32 s46, 0x8000\n"
      "v_add_u32 %[x0], s46, %[x0]\n"
      "v_add_u32 %[wa0], s46, %[wa0]\n"
      "v_add_u32 %[wa1], s46, %[wa1]\n"
      "v_mfma_f32_32x32x16_bf16 v[64:79], v[188:191], v[180:183], v[64:79]\n"
      "ds_read_b128 v[164:167], %[x0]\n"
      "ds_read_b64_tr_b16 v[172:173], %[wa0] offset:0\n"
      "ds_read_b64_tr_b16 v[174:175], %[wa0] offset:1024\n"
      "v_mfma_f32_32x32x16_bf16 v[80:95], v[192:195], v[180:183], v[80:95]\n"
      "ds_read_b64_tr_b16 v[176:177], %[wa1] offset:0\n"
      "ds_read_b64_tr_b16 v[178:179], %[wa1] offset:1024\n"
      "ds_read_b128 v[168:171], %[x0] offset:8192\n"
      "v_mfma_f32_32x32x16_bf16 v[96:111], v[188:191], v[184:187], v[96:111]\n"
      "s_add_u32 s44, %[m0g], 0\n"
      "s_mov_b32 m0, s44\n"
      "s_nop 0\n"
      "global_load_lds_dwordx4 %[o0], s[40:41] offset:0\n"
      "v_mfma_f32_32x32x16_bf16 v[112:127], v[192:195], v[184:187], v[112:127]\n"
      "global_load_lds_dwordx4 %[o1], s[40:41] offset:1024\n"
      "s_waitcnt lgkmcnt(3)\n"
      "v_add_u32 %[x1], s46, %[x1]\n"
      "v_mfma_f32_32x32x16_bf16 v[64:79], v[172:175], v[164:167], v[64:79]\n"
      "ds_read_b128 v[180:183], %[x1]\n"
      "ds_read_b64_tr_b16 v[188:189], %[wa0] offset:8192\n"
      "ds_read_b64_tr_b16 v[190:191], %[wa0] offset:9216\n"
      "s_waitcnt lgkmcnt(4)\n"
      "v_mfma_f32_32x32x16_bf16 v[80:95], v[176:179], v[164:167], v[80:95]\n"
      "ds_read_b64_tr_b16 v[192:193], %[wa1] offset:8192\n"
      "ds_read_b64_tr_b16 v[194:195], %[wa1] offset:9216\n"
      "ds_read_b128 v[184:187], %[x1] offset:8192\n"
      "s_waitcnt lgkmcnt(6)\n"
      "v_mfma_f32_32x32x16_bf16 v[96:111], v[172:175], v[168:171], v[96:111]\n"
      "global_load_lds_dwordx4 %[o2], s[42:43] offset:2048\n"
      "v_mfma_f32_32x32x16_bf16 v[112:127], v[176:179], v[168:171], v[112:127]\n"
      "global_load_lds_dwordx4 %[o3], s[42:43] offset:3072\n"
      "s_waitcnt lgkmcnt(3)\n"
      "v_add_u32 %[x2], s46, %[x2]\n"
      "v_mfma_f32_32x32x16_bf16 v[64:79], v[188:191], v[180:183], v[64:79]\n"
      "ds_read_b128 v[164:167], %[x2]\n"
      "ds_read_b64_tr_b16 v[172:173], %[wa0] offset:16384\n"
      "ds_read_b64_tr_b16 v[174:175], %[wa0] offset:17408\n"
      "s_waitcnt lgkmcnt(4)\n"
      "v_mfma_f32_32x32x16_bf16 v[80:95], v[192:195], v[180:183], v[80:95]\n"
      "ds_read_b64_tr_b16 v[176:177], %[wa1] offset:16384\n"
      "ds_read_b64_tr_b16 v[178:179], %[wa1] offset:17408\n"
      "ds_read_b128 v[168:171], %[x2] offset:8192\n"
      "s_waitcnt lgkmcnt(6)\n"
      "v_mfma_f32_32x32x16_bf16 v[96:111], v[188:191], v[184:187], v[96:111]\n"
      "s_add_u32 s44, %[m0g], 4096\n"
      "s_mov_b32 m0, s44\n"
      "s_nop 0\n"
      "global_load_lds_dwordx4 %[o4], s[40:41] offset:0\n"
      "v_mfma_f32_32x32x16_bf16 v[112:127], v[192:195], v[184:187], v[112:127]\n"
      "global_load_lds_dwordx4 %[o5], s[40:41] offset:1024\n"
      "s_waitcnt lgkmcnt(3)\n"
      "v_add_u32 %[x3], s46, %[x3]\n"
      "v_mfma_f32_32x32x16_bf16 v[64:79], v[172:175], v[164:167], v[64:79]\n"
      "ds_read_b128 v[180:183], %[x3]\n"
      "ds_read_b64_tr_b16 v[188:189], %[wa0] offset:24576\n"
      "ds_read_b64_tr_b16 v[190:191], %[wa0] offset:25600\n"
      "s_waitcnt lgkmcnt(4)\n"
      "v_mfma_f32_32x32x16_bf16 v[80:95], v[176:179], v[164:167], v[80:95]\n"
      "ds_read_b64_tr_b16 v[192:193], %[wa1] offset:24576\n"
      "ds_read_b64_tr_b16 v[194:195], %[wa1] offset:25600\n"
      "ds_read_b128 v[184:187], %[x3] offset:8192\n"
      "s_waitcnt lgkmcnt(6)\n"
      "v_mfma_f32_32x32x16_bf16 v[96:111], v[172:175], v[168:171], v[96:111]\n"
      "global_load_lds_dwordx4 %[o6], s[42:43] offset:2048\n"
      "v_mfma_f32_32x32x16_bf16 v[112:127], v[176:179], v[168:171], v[112:127]\n"
      "global_load_lds_dwordx4 %[o7], s[42:43] offset:3072\n"
      "s_add_u32 %[kt], %[kt], 1\n"
      "s_cmp_eq_u32 %[kt], 10\n"
      "s_cselect_b32 s44, 0xfffffb80, s48\n"
      "s_cselect_b32 s45, 0xffde4000, s49\n"
      "s_cselect_b32 %[kt], 0, %[kt]\n"
      "s_cselect_b32 s47, -1, 0\n"
      "s_add_u32 s40, s40, s44\n"
      "s_addc_u32 s41, s41, s47\n"
      "s_add_u32 s42, s42, s45\n"
      "s_addc_u32 s43, s43, s47\n"
      "s_waitcnt vmcnt(8) lgkmcnt(0)\n"
      "s_barrier\n"
      "s_mov_b32 s46, 0x8000\n"
      "v_add_u32 %[x0], s46, %[x0]\n"
      "v_add_u32 %[wa0], s46, %[wa0]\n"
      "v_add_u32 %[wa1], s46, %[wa1]\n"
      "v_mfma_f32_32x32x16_bf16 v[64:79], v[188:191], v[180:183], v[64:79]\n"
      "ds_read_b128 v[164:167], %[x0]\n"
      "ds_read_b64_tr_b16 v[172:173], %[wa0] offset:0\n"
      "ds_read_b64_tr_b16 v[174:175], %[wa0] offset:1024\n"
      "v_mfma_f32_32x32x16_bf16 v[80:95], v[192:195], v[180:183], v[80:95]\n"
      "ds_read_b64_tr_b16 v[176:177], %[wa1] offset:0\n"
      "ds_read_b64_tr_b16 v[178:179], %[wa1] offset:1024\n"
      "ds_read_b128 v[168:171], %[x0] offset:8192\n"
      "v_mfma_f32_32x32x16_bf16 v[96:111], v[188:191], v[184:187], v[96:111]\n"
      "s_add_u32 s44, %[m0g], 32768\n"
      "s_mov_b32 m0, s44\n"
      "s_nop 0\n"
      "global_load_lds_dwordx4 %[o0], s[40:41] offset:0\n"
      "v_mfma_f32_32x32x16_bf16 v[112:127], v[192:195], v[184:187], v[112:127]\n"
      "global_load_lds_dwordx4 %[o1], s[40:41] offset:1024\n"
      "s_waitcnt lgkmcnt(3)\n"
      "v_add_u32 %[x1], s46, %[x1]\n"
      "v_mfma_f32_32x32x16_bf16 v[64:79], v[172:175], v[164:167], v[64:79]\n"
      "ds_read_b128 v[180:183], %[x1]\n"
      "ds_read_b64_tr_b16 v[188:189], %[wa0] offset:8192\n"
      "ds_read_b64_tr_b16 v[190:191], %[wa0] offset:9216\n"
      "s_waitcnt lgkmcnt(4)\n"
      "v_mfma_f32_32x32x16_bf16 v[80:95], v[176:179], v[164:167], v[80:95]\n"
      "ds_read_b64_tr_b16 v[192:193], %[wa1] offset:8192\n"
      "ds_read_b64_tr_b16 v[194:195], %[wa1] offset:9216\n"
      "ds_read_b128 v[184:187], %[x1] offset:8192\n"
      "s_waitcnt lgkmcnt(6)\n"
      "v_mfma_f32_32x32x16_bf16 v[96:111], v[172:175], v[168:171], v[96:111]\n"
      "global_load_lds_dwordx4 %[o2], s[42:43] offset:2048\n"
      "v_mfma_f32_32x32x16_bf16 v[112:127], v[176:179], v[168:171], v[112:127]\n"
      "global_load_lds_dwordx4 %[o3], s[42:43] offset:3072\n"
      "s_waitcnt lgkmcnt(3)\n"
      "v_add_u32 %[x2], s46, %[x2]\n"
      "v_mfma_f32_32x32x16_bf16 v[64:79], v[188:191], v[180:183], v[64:79]\n"
      "ds_read_b128 v[164:167], %[x2]\n"
      "ds_read_b64_tr_b16 v[172:173], %[wa0] offset:16384\n"
      "ds_read_b64_tr_b16 v[174:175], %[wa0] offset:17408\n"
      "s_waitcnt lgkmcnt(4)\n"
      "v_mfma_f32_32x32x16_bf16 v[80:95], v[192:195], v[180:183], v[80:95]\n"
      "ds_read_b64_tr_b16 v[176:177], %[wa1] offset:16384\n"
      "ds_read_b64_tr_b16 v[178:179], %[wa1] offset:17408\n"
      "ds_read_b128 v[168:171], %[x2] offset:8192\n"
      "s_waitcnt lgkmcnt(6)\n"
      "v_mfma_f32_32x32x16_bf16 v[96:111], v[188:191], v[184:187], v[96:111]\n"
      "s_add_u32 s44, %[m0g], 36864\n"
      "s_mov_b32 m0, s44\n"
      "s_nop 0\n"
      "global_load_lds_dwordx4 %[o4], s[40:41] offset:0\n"
      "v_mfma_f32_32x32x16_bf16 v[112:127], v[192:195], v[184:187], v[112:127]\n"
      "global_load_lds_dwordx4 %[o5], s[40:41] offset:1024\n"
      "s_waitcnt lgkmcnt(3)\n"
      "v_add_u32 %[x3], s46, %[x3]\n"
      "v_mfma_f32_32x32x16_bf16 v[64:79], v[172:175], v[164:167], v[64:79]\n"
      "ds_read_b128 v[180:183], %[x3]\n"
      "ds_read_b64_tr_b16 v[188:189], %[wa0] offset:24576\n"
      "ds_read_b64_tr_b16 v[190:191], %[wa0] offset:25600\n"
      "s_waitcnt lgkmcnt(4)\n"
      "v_mfma_f32_32x32x16_bf16 v[80:95], v[176:179], v[164:167], v[80:95]\n"
      "ds_read_b64_tr_b16 v[192:193], %[wa1] offset:24576\n"
      "ds_read_b64_tr_b16 v[194:195], %[wa1] offset:25600\n"
      "ds_read_b128 v[184:187], %[x3] offset:8192\n"
      "s_waitcnt lgkmcnt(6)\n"
      "v_mfma_f32_32x32x16_bf16 v[96:111], v[172:175], v[168:171], v[96:111]\n"
      "global_load_lds_dwordx4 %[o6], s[42:43] offset:2048\n"
      "v_mfma_f32_32x32x16_bf16 v[112:127], v[176:179], v[168:171], v[112:127]\n"
      "global_load_lds_dwordx4 %[o7], s[42:43] offset:3072\n"
      "s_add_u32 %[kt], %[kt], 1\n"
      "s_cmp_eq_u32 %[kt], 10\n"
      "s_cselect_b32 s44, 0xfffffb80, s48\n"
      "s_cselect_b32 s45, 0xffde4000, s49\n"
      "s_cselect_b32 %[kt], 0, %[kt]\n"
      "s_cselect_b32 s47, -1, 0\n"
      "s_add_u32 s40, s40, s44\n"
      "s_addc_u32 s41, s41, s47\n"
      "s_add_u32 s42, s42, s45\n"
      "s_addc_u32 s43, s43, s47\n"
      "s_waitcnt vmcnt(8) lgkmcnt(0)\n"
      "s_barrier\n"
      "s_mov_b32 s46, 0xffff0000\n"
      "v_add_u32 %[x0], s46, %[x0]\n"
      "v_add_u32 %[wa0], s46, %[wa0]\n"
      "v_add_u32 %[wa1], s46, %[wa1]\n"
      "v_mfma_f32_32x32x16_bf16 v[64:79], v[188:191], v[180:183], v[64:79]\n"
      "ds_read_b128 v[164:167], %[x0]\n"
      "ds_read_b64_tr_b16 v[172:173], %[wa0] offset:0\n"
      "ds_read_b64_tr_b16 v[174:175], %[wa0] offset:1024\n"
      "v_mfma_f32_32x32x16_bf16 v[80:95], v[192:195], v[180:183], v[80:95]\n"
      "ds_read_b64_tr_b16 v[176:177], %[wa1] offset:0\n"
      "ds_read_b64_tr_b16 v[178:179], %[wa1] offset:1024\n"
      "ds_read_b128 v[168:171], %[x0] offset:8192\n"
      "v_mfma_f32_32x32x16_bf16 v[96:111], v[188:191], v[184:187], v[96:111]\n"
      "s_add_u32 s44, %[m0g], 65536\n"
      "s_mov_b32 m0, s44\n"
      "s_nop 0\n"
      "global_load_lds_dwordx4 %[o0], s[40:41] offset:0\n"
      "v_mfma_f32_32x32x16_bf16 v[112:127], v[192:195], v[184:187], v[112:127]\n"
      "global_load_lds_dwordx4 %[o1], s[40:41] offset:1024\n"
      "s_waitcnt lgkmcnt(3)\n"
      "v_add_u32 %[x1], s46, %[x1]\n"
      "v_mfma_f32_32x32x16_bf16 v[64:79], v[172:175], v[164:167], v[64:79]\n"
      "ds_read_b128 v[180:183], %[x1]\n"
      "ds_read_b64_tr_b16 v[188:189], %[wa0] offset:8192\n"
      "ds_read_b64_tr_b16 v[190:191], %[wa0] offset:9216\n"
      "s_waitcnt lgkmcnt(4)\n"
      "v_mfma_f32_32x32x16_bf16 v[80:95], v[176:179], v[164:167], v[80:95]\n"
      "ds_read_b64_tr_b16 v[192:193], %[wa1] offset:8192\n"
      "ds_read_b64_tr_b16 v[194:195], %[wa1] offset:9216\n"
      "ds_read_b128 v[184:187], %[x1] offset:8192\n"
      "s_waitcnt lgkmcnt(6)\n"
      "v_mfma_f32_32x32x16_bf16 v[96:111], v[172:175], v[168:171], v[96:111]\n"
      "global_load_lds_dwordx4 %[o2], s[42:43] offset:2048\n"
      "v_mfma_f32_32x32x16_bf16 v[112:127], v[176:179], v[168:171], v[112:127]\n"
      "global_load_lds_dwordx4 %[o3], s[42:43] offset:3072\n"
      "s_waitcnt lgkmcnt(3)\n"
      "v_add_u32 %[x2], s46, %[x2]\n"
      "v_mfma_f32_32x32x16_bf16 v[64:79], v[188:191], v[180:183], v[64:79]\n"
      "ds_read_b128 v[164:167], %[x2]\n"
      "ds_read_b64_tr_b16 v[172:173], %[wa0] offset:16384\n"
      "ds_read_b64_tr_b16 v[174:175], %[wa0] offset:17408\n"
      "s_waitcnt lgkmcnt(4)\n"
      "v_mfma_f32_32x32x16_bf16 v[80:95], v[192:195], v[180:183], v[80:95]\n"
      "ds_read_b64_tr_b16 v[176:177], %[wa1] offset:16384\n"
      "ds_read_b64_tr_b16 v[178:179], %[wa1] offset:17408\n"
      "ds_read_b128 v[168:171], %[x2] offset:8192\n"
      "s_waitcnt lgkmcnt(6)\n"
      "v_mfma_f32_32x32x16_bf16 v[96:111], v[188:191], v[184:187], v[96:111]\n"
      "s_add_u32 s44, %[m0g], 69632\n"
      "s_mov_b32 m0, s44\n"
      "s_nop 0\n"
      "global_load_lds_dwordx4 %[o4], s[40:41] offset:0\n"
      "v_mfma_f32_32x32x16_bf16 v[112:127], v[192:195], v[184:187], v[112:127]\n"
      "global_load_lds_dwordx4 %[o5], s[40:41] offset:1024\n"
      "s_waitcnt lgkmcnt(3)\n"
      "v_add_u32 %[x3], s46, %[x3]\n"
      "v_mfma_f32_32x32x16_bf16 v[64:79], v[172:175], v[164:167], v[64:79]\n"
      "ds_read_b128 v[180:183], %[x3]\n"
      "ds_read_b64_tr_b16 v[188:189], %[wa0] offset:24576\n"
      "ds_read_b64_tr_b16 v[190:191], %[wa0] offset:25600\n"
      "s_waitcnt lgkmcnt(4)\n"
      "v_mfma_f32_32x32x16_bf16 v[80:95], v[176:179], v[164:167], v[80:95]\n"
      "ds_read_b64_tr_b16 v[192:193], %[wa1] offset:24576\n"
      "ds_read_b64_tr_b16 v[194:195], %[wa1] offset:25600\n"
      "ds_read_b128 v[184:187], %[x3] offset:8192\n"
      "s_waitcnt lgkmcnt(6)\n"
      "v_mfma_f32_32x32x16_bf16 v[96:111], v[172:175], v[168:171], v[96:111]\n"
      "global_load_lds_dwordx4 %[o6], s[42:43] offset:2048\n"
      "v_mfma_f32_32x32x16_bf16 v[112:127], v[176:179], v[168:171], v[112:127]\n"
      "global_load_lds_dwordx4 %[o7], s[42:43] offset:3072\n"
      "s_add_u32 %[kt], %[kt], 1\n"
      "s_cmp_eq_u32 %[kt], 10\n"
      "s_cselect_b32 s44, 0xfffffb80, s48\n"
      "s_cselect_b32 s45, 0xffde4000, s49\n"
      "s_cselect_b32 %[kt], 0, %[kt]\n"
      "s_cselect_b32 s47, -1, 0\n"
      "s_add_u32 s40, s40, s44\n"
      "s_addc_u32 s41, s41, s47\n"
      "s_add_u32 s42, s42, s45\n"
      "s_addc_u32 s43, s43, s47\n"
      "s_waitcnt vmcnt(8) lgkmcnt(0)\n"
      "s_barrier\n"
      "s_mov_b32 s46, 0x8000\n"
      "v_add_u32 %[x0], s46, %[x0]\n"
      "v_add_u32 %[wa0], s46, %[wa0]\n"
      "v_add_u32 %[wa1], s46, %[wa1]\n"
      "v_mfma_f32_32x32x16_bf16 v[64:79], v[188:191], v[180:183], v[64:79]\n"
      "ds_read_b128 v[164:167], %[x0]\n"
      "ds_read_b64_tr_b16 v[172:173], %[wa0] offset:0\n"
      "ds_read_b64_tr_b16 v[174:175], %[wa0] offset:1024\n"
      "v_mfma_f32_32x32x16_bf16 v[80:95], v[192:195], v[180:183], v[80:95]\n"
      "ds_read_b64_tr_b16 v[176:177], %[wa1] offset:0\n"
      "ds_read_b64_tr_b16 v[178:179], %[wa1] offset:1024\n"
      "ds_read_b128 v[168:171], %[x0] offset:8192\n"
      "v_mfma_f32_32x32x16_bf16 v[96:111], v[188:191], v[184:187], v[96:111]\n"
      "s_add_u32 s44, %[m0g], 0\n"
      "s_mov_b32 m0, s44\n"
      "s_nop 0\n"
      "global_load_lds_dwordx4 %[o0], s[40:41] offset:0\n"
      "v_mfma_f32_32x32x16_bf16 v[112:127], v[192:195], v[184:187], v[112:127]\n"
      "global_load_lds_dwordx4 %[o1], s[40:41] offset:1024\n"
      "s_waitcnt lgkmcnt(3)\n"
      "v_add_u32 %[x1], s46, %[x1]\n"
      "v_mfma_f32_32x32x16_bf16 v[64:79], v[172:175], v[164:167], v[64:79]\n"
      "ds_read_b128 v[180:183], %[x1]\n"
      "ds_read_b64_tr_b16 v[188:189], %[wa0] offset:8192\n"
      "ds_read_b64_tr_b16 v[190:191], %[wa0] offset:9216\n"
      "s_waitcnt lgkmcnt(4)\n"
      "v_mfma_f32_32x32x16_bf16 v[80:95], v[176:179], v[164:167], v[80:95]\n"
      "ds_read_b64_tr_b16 v[192:193], %[wa1] offset:8192\n"
      "ds_read_b64_tr_b16 v[194:195], %[wa1] offset:9216\n"
      "ds_read_b128 v[184:187], %[x1] offset:8192\n"
      "s_waitcnt lgkmcnt(6)\n"
      "v_mfma_f32_32x32x16_bf16 v[96:111], v[172:175], v[168:171], v[96:111]\n"
      "global_load_lds_dwordx4 %[o2], s[42:43] offset:2048\n"
      "v_mfma_f32_32x32x16_bf16 v[112:127], v[176:179], v[168:171], v[112:127]\n"
      "global_load_lds_dwordx4 %[o3], s[42:43] offset:3072\n"
      "s_waitcnt lgkmcnt(3)\n"
      "v_add_u32 %[x2], s46, %[x2]\n"
      "v_mfma_f32_32x32x16_bf16 v[64:79], v[188:191], v[180:183], v[64:79]\n"
      "ds_read_b128 v[164:167], %[x2]\n"
      "ds_read_b64_tr_b16 v[172:173], %[wa0] offset:16384\n"
      "ds_read_b64_tr_b16 v[174:175], %[wa0] offset:17408\n"
      "s_waitcnt lgkmcnt(4)\n"
      "v_mfma_f32_32x32x16_bf16 v[80:95], v[192:195], v[180:183], v[80:95]\n"
      "ds_read_b64_tr_b16 v[176:177], %[wa1] offset:16384\n"
      "ds_read_b64_tr_b16 v[178:179], %[wa1] offset:17408\n"
      "ds_read_b128 v[168:171], %[x2] offset:8192\n"
      "s_waitcnt lgkmcnt(6)\n"
      "v_mfma_f32_32x32x16_bf16 v[96:111], v[188:191], v[184:187], v[96:111]\n"
      "s_add_u32 s44, %[m0g], 4096\n"
      "s_mov_b32 m0, s44\n"
      "s_nop 0\n"
      "global_load_lds_dwordx4 %[o4], s[40:41] offset:0\n"
      "v_mfma_f32_32x32x16_bf16 v[112:127], v[192:195], v[184:187], v[112:127]\n"
      "global_load_lds_dwordx4 %[o5], s[40:41] offset:1024\n"
      "s_waitcnt lgkmcnt(3)\n"
      "v_add_u32 %[x3], s46, %[x3]\n"
      "v_mfma_f32_32x32x16_bf16 v[64:79], v[172:175], v[164:167], v[64:79]\n"
      "ds_read_b128 v[180:183], %[x3]\n"
      "ds_read_b64_tr_b16 v[188:189], %[wa0] offset:24576\n"
      "ds_read_b64_tr_b16 v[190:191], %[wa0] offset:25600\n"
      "s_waitcnt lgkmcnt(4)\n"
      "v_mfma_f32_32x32x16_bf16 v[80:95], v[176:179], v[164:167], v[80:95]\n"
      "ds_read_b64_tr_b16 v[192:193], %[wa1] offset:24576\n"
      "ds_read_b64_tr_b16 v[194:195], %[wa1] offset:25600\n"
      "ds_read_b128 v[184:187], %[x3] offset:8192\n"
      "s_waitcnt lgkmcnt(6)\n"
      "v_mfma_f32_32x32x16_bf16 v[96:111], v[172:175], v[168:171], v[96:111]\n"
      "global_load_lds_dwordx4 %[o6], s[42:43] offset:2048\n"
      "v_mfma_f32_32x32x16_bf16 v[112:127], v[176:179], v[168:171], v[112:127]\n"
      "global_load_lds_dwordx4 %[o7], s[42:43] offset:3072\n"
      "s_add_u32 %[kt], %[kt], 1\n"
      "s_cmp_eq_u32 %[kt], 10\n"
      "s_cselect_b32 s44, 0xfffffb80, s48\n"
      "s_cselect_b32 s45, 0xffde4000, s49\n"
      "s_cselect_b32 %[kt], 0, %[kt]\n"
      "s_cselect_b32 s47, -1, 0\n"
      "s_add_u32 s40, s40, s44\n"
      "s_addc_u32 s41, s41, s47\n"
      "s_add_u32 s42, s42, s45\n"
      "s_addc_u32 s43, s43, s47\n"
      "s_waitcnt vmcnt(8) lgkmcnt(0)\n"
      "s_barrier\n"
      "s_mov_b32 s46, 0x8000\n"
      "v_add_u32 %[x0], s46, %[x0]\n"
      "v_add_u32 %[wa0], s46, %[wa0]\n"
      "v_add_u32 %[wa1], s46, %[wa1]\n"
      "v_mfma_f32_32x32x16_bf16 v[64:79], v[188:191], v[180:183], v[64:79]\n"
      "ds_read_b128 v[164:167], %[x0]\n"
      "ds_read_b64_tr_b16 v[172:173], %[wa0] offset:0\n"
      "ds_read_b64_tr_b16 v[174:175], %[wa0] offset:1024\n"
      "v_mfma_f32_32x32x16_bf16 v[80:95], v[192:195], v[180:183], v[80:95]\n"
      "ds_read_b64_tr_b16 v[176:177], %[wa1] offset:0\n"
      "ds_read_b64_tr_b16 v[178:179], %[wa1] offset:1024\n"
      "ds_read_b128 v[168:171], %[x0] offset:8192\n"
      "v_mfma_f32_32x32x16_bf16 v[96:111], v[188:191], v[184:187], v[96:111]\n"
      "s_add_u32 s44, %[m0g], 32768\n"
      "s_mov_b32 m0, s44\n"
      "s_nop 0\n"
      "global_load_lds_dwordx4 %[o0], s[40:41] offset:0\n"
      "v_mfma_f32_32x32x16_bf16 v[112:127], v[192:195], v[184:187], v[112:127]\n"
      "global_load_lds_dwordx4 %[o1], s[40:41] offset:1024\n"
      "s_waitcnt lgkmcnt(3)\n"
      "v_add_u32 %[x1], s46, %[x1]\n"
      "v_mfma_f32_32x32x16_bf16 v[64:79], v[172:175], v[164:167], v[64:79]\n"
      "ds_read_b128 v[180:183], %[x1]\n"
      "ds_read_b64_tr_b16 v[188:189], %[wa0] offset:8192\n"
      "ds_read_b64_tr_b16 v[190:191], %[wa0] offset:9216\n"
      "s_waitcnt lgkmcnt(4)\n"
      "v_mfma_f32_32x32x16_bf16 v[80:95], v[176:179], v[164:167], v[80:95]\n"
      "ds_read_b64_tr_b16 v[192:193], %[wa1] offset:8192\n"
      "ds_read_b64_tr_b16 v[194:195], %[wa1] offset:9216\n"
      "ds_read_b128 v[184:187], %[x1] offset:8192\n"
      "s_waitcnt lgkmcnt(6)\n"
      "v_mfma_f32_32x32x16_bf16 v[96:111], v[172:175], v[168:171], v[96:111]\n"
      "global_load_lds_dwordx4 %[o2], s[42:43] offset:2048\n"
      "v_mfma_f32_32x32x16_bf16 v[112:127], v[176:179], v[168:171], v[112:127]\n"
      "global_load_lds_dwordx4 %[o3], s[42:43] offset:3072\n"
      "s_waitcnt lgkmcnt(3)\n"
      "v_add_u32 %[x2], s46, %[x2]\n"
      "v_mfma_f32_32x32x16_bf16 v[64:79], v[188:191], v[180:183], v[64:79]\n"
      "ds_read_b128 v[164:167], %[x2]\n"
      "ds_read_b64_tr_b16 v[172:173], %[wa0] offset:16384\n"
      "ds_read_b64_tr_b16 v[174:175], %[wa0] offset:17408\n"
      "s_waitcnt lgkmcnt(4)\n"
      "v_mfma_f32_32x32x16_bf16 v[80:95], v[192:195], v[180:183], v[80:95]\n"
      "ds_read_b64_tr_b16 v[176:177], %[wa1] offset:16384\n"
      "ds_read_b64_tr_b16 v[178:179], %[wa1] offset:17408\n"
      "ds_read_b128 v[168:171], %[x2] offset:8192\n"
      "s_waitcnt lgkmcnt(6)\n"
      "v_mfma_f32_32x32x16_bf16 v[96:111], v[188:191], v[184:187], v[96:111]\n"
      "s_add_u32 s44, %[m0g], 36864\n"
      "s_mov_b32 m0, s44\n"
      "s_nop 0\n"
      "global_load_lds_dwordx4 %[o4], s[40:41] offset:0\n"
      "v_mfma_f32_32x32x16_bf16 v[112:127], v[192:195], v[184:187], v[112:127]\n"
      "global_load_lds_dwordx4 %[o5], s[40:41] offset:1024\n"
      "s_waitcnt lgkmcnt(3)\n"
      "v_add_u32 %[x3], s46, %[x3]\n"
      "v_mfma_f32_32x32x16_bf16 v[64:79], v[172:175], v[164:167], v[64:79]\n"
      "ds_read_b128 v[180:183], %[x3]\n"
      "ds_read_b64_tr_b16 v[188:189], %[wa0] offset:24576\n"
      "ds_read_b64_tr_b16 v[190:191], %[wa0] offset:25600\n"
      "s_waitcnt lgkmcnt(4)\n"
      "v_mfma_f32_32x32x16_bf16 v[80:95], v[176:179], v[164:167], v[80:95]\n"
      "ds_read_b64_tr_b16 v[192:193], %[wa1] offset:24576\n"
      "ds_read_b64_tr_b16 v[194:195], %[wa1] offset:25600\n"
      "ds_read_b128 v[184:187], %[x3] offset:8192\n"
      "s_waitcnt lgkmcnt(6)\n"
      "v_mfma_f32_32x32x16_bf16 v[96:111], v[172:175], v[168:171], v[96:111]\n"
      "global_load_lds_dwordx4 %[o6], s[42:43] offset:2048\n"
      "v_mfma_f32_32x32x16_bf16 v[112:127], v[176:179], v[168:171], v[112:127]\n"
      "global_load_lds_dwordx4 %[o7], s[42:43] offset:3072\n"
      "s_add_u32 %[kt], %[kt], 1\n"
      "s_cmp_eq_u32 %[kt], 10\n"
      "s_cselect_b32 s44, 0xfffffb80, s48\n"
      "s_cselect_b32 s45, 0xffde4000, s49\n"
      "s_cselect_b32 %[kt], 0, %[kt]\n"
      "s_cselect_b32 s47, -1, 0\n"
      "s_add_u32 s40, s40, s44\n"
      "s_addc_u32 s41, s41, s47\n"
      "s_add_u32 s42, s42, s45\n"
      "s_addc_u32 s43, s43, s47\n"
      "s_waitcnt vmcnt(8) lgkmcnt(0)\n"
      "s_barrier\n"
      "s_mov_b32 s46, 0xffff0000\n"
      "v_add_u32 %[x0], s46, %[x0]\n"
      "v_add_u32 %[wa0], s46, %[wa0]\n"
      "v_add_u32 %[wa1], s46, %[wa1]\n"
      "v_mfma_f32_32x32x16_bf16 v[64:79], v[188:191], v[180:183], v[64:79]\n"
      "ds_read_b128 v[164:167], %[x0]\n"
      "ds_read_b64_tr_b16 v[172:173], %[wa0] offset:0\n"
      "ds_read_b64_tr_b16 v[174:175], %[wa0] offset:1024\n"
      "v_mfma_f32_32x32x16_bf16 v[80:95], v[192:195], v[180:183], v[80:95]\n"
      "ds_read_b64_tr_b16 v[176:177], %[wa1] offset:0\n"
      "ds_read_b64_tr_b16 v[178:179], %[wa1] offset:1024\n"
      "ds_read_b128 v[168:171], %[x0] offset:8192\n"
      "v_mfma_f32_32x32x16_bf16 v[96:111], v[188:191], v[184:187], v[96:111]\n"
      "s_add_u32 s44, %[m0g], 65536\n"
      "s_mov_b32 m0, s44\n"
      "s_nop 0\n"
      "global_load_lds_dwordx4 %[o0], s[40:41] offset:0\n"
      "v_mfma_f32_32x32x16_bf16 v[112:127], v[192:195], v[184:187], v[112:127]\n"
      "global_load_lds_dwordx4 %[o1], s[40:41] offset:1024\n"
      "s_waitcnt lgkmcnt(3)\n"
      "v_add_u32 %[x1], s46, %[x1]\n"
      "v_mfma_f32_32x32x16_bf16 v[64:79], v[172:175], v[164:167], v[64:79]\n"
      "ds_read_b128 v[180:183], %[x1]\n"
      "ds_read_b64_tr_b16 v[188:189], %[wa0] offset:8192\n"
      "ds_read_b64_tr_b16 v[190:191], %[wa0] offset:9216\n"
      "s_waitcnt lgkmcnt(4)\n"
      "v_mfma_f32_32x32x16_bf16 v[80:95], v[176:179], v[164:167], v[80:95]\n"
      "ds_read_b64_tr_b16 v[192:193], %[wa1] offset:8192\n"
      "ds_read_b64_tr_b16 v[194:195], %[wa1] offset:9216\n"
      "ds_read_b128 v[184:187], %[x1] offset:8192\n"
      "s_waitcnt lgkmcnt(6)\n"
      "v_mfma_f32_32x32x16_bf16 v[96:111], v[172:175], v[168:171], v[96:111]\n"
      "global_load_lds_dwordx4 %[o2], s[42:43] offset:2048\n"
      "v_mfma_f32_32x32x16_bf16 v[112:127], v[176:179], v[168:171], v[112:127]\n"
      "global_load_lds_dwordx4 %[o3], s[42:43] offset:3072\n"
      "s_waitcnt lgkmcnt(3)\n"
      "v_add_u32 %[x2], s46, %[x2]\n"
      "v_mfma_f32_32x32x16_bf16 v[64:79], v[188:191], v[180:183], v[64:79]\n"
      "ds_read_b128 v[164:167], %[x2]\n"
      "ds_read_b64_tr_b16 v[172:173], %[wa0] offset:16384\n"
      "ds_read_b64_tr_b16 v[174:175], %[wa0] offset:17408\n"
      "s_waitcnt lgkmcnt(4)\n"
      "v_mfma_f32_32x32x16_bf16 v[80:95], v[192:195], v[180:183], v[80:95]\n"
      "ds_read_b64_tr_b16 v[176:177], %[wa1] offset:16384\n"
      "ds_read_b64_tr_b16 v[178:179], %[wa1] offset:17408\n"
      "ds_read_b128 v[168:171], %[x2] offset:8192\n"
      "s_waitcnt lgkmcnt(6)\n"
      "v_mfma_f32_32x32x16_bf16 v[96:111], v[188:191], v[184:187], v[96:111]\n"
      "s_add_u32 s44, %[m0g], 69632\n"
      "s_mov_b32 m0, s44\n"
      "s_nop 0\n"
      "global_load_lds_dwordx4 %[o4], s[40:41] offset:0\n"
      "v_mfma_f32_32x32x16_bf16 v[112:127], v[192:195], v[184:187], v[112:127]\n"
      "global_load_lds_dwordx4 %[o5], s[40:41] offset:1024\n"
      "s_waitcnt lgkmcnt(3)\n"
      "v_add_u32 %[x3], s46, %[x3]\n"
      "v_mfma_f32_32x32x16_bf16 v[64:79], v[172:175], v[164:167], v[64:79]\n"
      "ds_read_b128 v[180:183], %[x3]\n"
      "ds_read_b64_tr_b16 v[188:189], %[wa0] offset:24576\n"
      "ds_read_b64_tr_b16 v[190:191], %[wa0] offset:25600\n"
      "s_waitcnt lgkmcnt(4)\n"
      "v_mfma_f32_32x32x16_bf16 v[80:95], v[176:179], v[164:167], v[80:95]\n"
      "ds_read_b64_tr_b16 v[192:193], %[wa1] offset:24576\n"
      "ds_read_b64_tr_b16 v[194:195], %[wa1] offset:25600\n"
      "ds_read_b128 v[184:187], %[x3] offset:8192\n"
      "s_waitcnt lgkmcnt(6)\n"
      "v_mfma_f32_32x32x16_bf16 v[96:111], v[172:175], v[168:171], v[96:111]\n"
      "global_load_lds_dwordx4 %[o6], s[42:43] offset:2048\n"
      "v_mfma_f32_32x32x16_bf16 v[112:127], v[176:179], v[168:171], v[112:127]\n"
      "global_load_lds_dwordx4 %[o7], s[42:43] offset:3072\n"
      "s_add_u32 %[kt], %[kt], 1\n"
      "s_cmp_eq_u32 %[kt], 10\n"
      "s_cselect_b32 s44, 0xfffffb80, s48\n"
      "s_cselect_b32 s45, 0xffde4000, s49\n"
      "s_cselect_b32 %[kt], 0, %[kt]\n"
      "s_cselect_b32 s47, -1, 0\n"
      "s_add_u32 s40, s40, s44\n"
      "s_addc_u32 s41, s41, s47\n"
      "s_add_u32 s42, s42, s45\n"
      "s_addc_u32 s43, s43, s47\n"
      "s_waitcnt vmcnt(8) lgkmcnt(0)\n"
      "s_barrier\n"
      "s_mov_b32 s46, 0x8000\n"
      "v_add_u32 %[x0], s46, %[x0]\n"
      "v_add_u32 %[wa0], s46, %[wa0]\n"
      "v_add_u32 %[wa1], s46, %[wa1]\n"
      "v_mfma_f32_32x32x16_bf16 v[64:79], v[188:191], v[180:183], v[64:79]\n"
      "ds_read_b128 v[164:167], %[x0]\n"
      "ds_read_b64_tr_b16 v[172:173], %[wa0] offset:0\n"
      "ds_read_b64_tr_b16 v[174:175], %[wa0] offset:1024\n"
      "v_mfma_f32_32x32x16_bf16 v[80:95], v[192:195], v[180:183], v[80:95]\n"
      "ds_read_b64_tr_b16 v[176:177], %[wa1] offset:0\n"
      "ds_read_b64_tr_b16 v[178:179], %[wa1] offset:1024\n"
      "ds_read_b128 v[168:171], %[x0] offset:8192\n"
      "v_mfma_f32_32x32x16_bf16 v[96:111], v[188:191], v[184:187], v[96:111]\n"
      "s_add_u32 s44, %[m0g], 0\n"
      "s_mov_b32 m0, s44\n"
      "s_nop 0\n"
      "global_load_lds_dwordx4 %[o0], s[40:41] offset:0\n"
      "v_mfma_f32_32x32x16_bf16 v[112:127], v[192:195], v[184:187], v[112:127]\n"
      "global_load_lds_dwordx4 %[o1], s[40:41] offset:1024\n"
      "s_waitcnt lgkmcnt(3)\n"
      "v_add_u32 %[x1], s46, %[x1]\n"
      "v_mfma_f32_32x32x16_bf16 v[64:79], v[172:175], v[164:167], v[64:79]\n"
      "ds_read_b128 v[180:183], %[x1]\n"
      "ds_read_b64_tr_b16 v[188:189], %[wa0] offset:8192\n"
      "ds_read_b64_tr_b16 v[190:191], %[wa0] offset:9216\n"
      "s_waitcnt lgkmcnt(4)\n"
      "v_mfma_f32_32x32x16_bf16 v[80:95], v[176:179], v[164:167], v[80:95]\n"
      "ds_read_b64_tr_b16 v[192:193], %[wa1] offset:8192\n"
      "ds_read_b64_tr_b16 v[194:195], %[wa1] offset:9216\n"
      "ds_read_b128 v[184:187], %[x1] offset:8192\n"
      "s_waitcnt lgkmcnt(6)\n"
      "v_mfma_f32_32x32x16_bf16 v[96:111], v[172:175], v[168:171], v[96:111]\n"
      "global_load_lds_dwordx4 %[o2], s[42:43] offset:2048\n"
      "v_mfma_f32_32x32x16_bf16 v[112:127], v[176:179], v[168:171], v[112:127]\n"
      "global_load_lds_dwordx4 %[o3], s[42:43] offset:3072\n"
      "s_waitcnt lgkmcnt(3)\n"
      "v_add_u32 %[x2], s46, %[x2]\n"
      "v_mfma_f32_32x32x16_bf16 v[64:79], v[188:191], v[180:183], v[64:79]\n"
      "ds_read_b128 v[164:167], %[x2]\n"
      "ds_read_b64_tr_b16 v[172:173], %[wa0] offset:16384\n"
      "ds_read_b64_tr_b16 v[174:175], %[wa0] offset:17408\n"
      "s_waitcnt lgkmcnt(4)\n"
      "v_mfma_f32_32x32x16_bf16 v[80:95], v[192:195], v[180:183], v[80:95]\n"
      "ds_read_b64_tr_b16 v[176:177], %[wa1] offset:16384\n"
      "ds_read_b64_tr_b16 v[178:179], %[wa1] offset:17408\n"
      "ds_read_b128 v[168:171], %[x2] offset:8192\n"
      "s_waitcnt lgkmcnt(6)\n"
      "v_mfma_f32_32x32x16_bf16 v[96:111], v[188:191], v[184:187], v[96:111]\n"
      "s_add_u32 s44, %[m0g], 4096\n"
      "s_mov_b32 m0, s44\n"
      "s_nop 0\n"
      "global_load_lds_dwordx4 %[o4], s[40:41] offset:0\n"
      "v_mfma_f32_32x32x16_bf16 v[112:127], v[192:195], v[184:187], v[112:127]\n"
      "global_load_lds_dwordx4 %[o5], s[40:41] offset:1024\n"
      "s_waitcnt lgkmcnt(3)\n"
      "v_add_u32 %[x3], s46, %[x3]\n"
      "v_mfma_f32_32x32x16_bf16 v[64:79], v[172:175], v[164:167], v[64:79]\n"
      "ds_read_b128 v[180:183], %[x3]\n"
      "ds_read_b64_tr_b16 v[188:189], %[wa0] offset:24576\n"
      "ds_read_b64_tr_b16 v[190:191], %[wa0] offset:25600\n"
      "s_waitcnt lgkmcnt(4)\n"
      "v_mfma_f32_32x32x16_bf16 v[80:95], v[176:179], v[164:167], v[80:95]\n"
      "ds_read_b64_tr_b16 v[192:193], %[wa1] offset:24576\n"
      "ds_read_b64_tr_b16 v[194:195], %[wa1] offset:25600\n"
      "ds_read_b128 v[184:187], %[x3] offset:8192\n"
      "s_waitcnt lgkmcnt(6)\n"
      "v_mfma_f32_32x32x16_bf16 v[96:111], v[172:175], v[168:171], v[96:111]\n"
      "global_load_lds_dwordx4 %[o6], s[42:43] offset:2048\n"
      "v_mfma_f32_32x32x16_bf16 v[112:127], v[176:179], v[168:171], v[112:127]\n"
      "global_load_lds_dwordx4 %[o7], s[42:43] offset:3072\n"
      "s_waitcnt vmcnt(8) lgkmcnt(0)\n"
      "s_barrier\n"
      "s_mov_b32 s46, 0x8000\n"
      "v_add_u32 %[x0], s46, %[x0]\n"
      "v_add_u32 %[wa0], s46, %[wa0]\n"
      "v_add_u32 %[wa1], s46, %[wa1]\n"
      "v_mfma_f32_32x32x16_bf16 v[64:79], v[188:191], v[180:183], v[64:79]\n"
      "ds_read_b128 v[164:167], %[x0]\n"
      "ds_read_b64_tr_b16 v[172:173], %[wa0] offset:0\n"
      "ds_read_b64_tr_b16 v[174:175], %[wa0] offset:1024\n"
      "v_mfma_f32_32x32x16_bf16 v[80:95], v[192:195], v[180:183], v[80:95]\n"
      "ds_read_b64_tr_b16 v[176:177], %[wa1] offset:0\n"
      "ds_read_b64_tr_b16 v[178:179], %[wa1] offset:1024\n"
      "ds_read_b128 v[168:171], %[x0] offset:8192\n"
      "v_mfma_f32_32x32x16_bf16 v[96:111], v[188:191], v[184:187], v[96:111]\n"
      "v_mfma_f32_32x32x16_bf16 v[112:127], v[192:195], v[184:187], v[112:127]\n"
      "s_waitcnt lgkmcnt(3)\n"
      "v_add_u32 %[x1], s46, %[x1]\n"
      "v_mfma_f32_32x32x16_bf16 v[64:79], v[172:175], v[164:167], v[64:79]\n"
      "ds_read_b128 v[180:183], %[x1]\n"
      "ds_read_b64_tr_b16 v[188:189], %[wa0] offset:8192\n"
      "ds_read_b64_tr_b16 v[190:191], %[wa0] offset:9216\n"
      "s_waitcnt lgkmcnt(4)\n"
      "v_mfma_f32_32x32x16_bf16 v[80:95], v[176:179], v[164:167], v[80:95]\n"
      "ds_read_b64_tr_b16 v[192:193], %[wa1] offset:8192\n"
      "ds_read_b64_tr_b16 v[194:195], %[wa1] offset:9216\n"
      "ds_read_b128 v[184:187], %[x1] offset:8192\n"
      "s_waitcnt lgkmcnt(6)\n"
      "v_mfma_f32_32x32x16_bf16 v[96:111], v[172:175], v[168:171], v[96:111]\n"
      "v_mfma_f32_32x32x16_bf16 v[112:127], v[176:179], v[168:171], v[112:127]\n"
      "s_waitcnt lgkmcnt(3)\n"
      "v_add_u32 %[x2], s46, %[x2]\n"
      "v_mfma_f32_32x32x16_bf16 v[64:79], v[188:191], v[180:183], v[64:79]\n"
      "ds_read_b128 v[164:167], %[x2]\n"
      "ds_read_b64_tr_b16 v[172:173], %[wa0] offset:16384\n"
      "ds_read_b64_tr_b16 v[174:175], %[wa0] offset:17408\n"
      "s_waitcnt lgkmcnt(4)\n"
      "v_mfma_f32_32x32x16_bf16 v[80:95], v[192:195], v[180:183], v[80:95]\n"
      "ds_read_b64_tr_b16 v[176:177], %[wa1] offset:16384\n"
      "ds_read_b64_tr_b16 v[178:179], %[wa1] offset:17408\n"
      "ds_read_b128 v[168:171], %[x2] offset:8192\n"
      "s_waitcnt lgkmcnt(6)\n"
      "v_mfma_f32_32x32x16_bf16 v[96:111], v[188:191], v[184:187], v[96:111]\n"
      "v_mfma_f32_32x32x16_bf16 v[112:127], v[192:195], v[184:187], v[112:127]\n"
      "s_waitcnt lgkmcnt(3)\n"
      "v_add_u32 %[x3], s46, %[x3]\n"
      "v_mfma_f32_32x32x16_bf16 v[64:79], v[172:175], v[164:167], v[64:79]\n"
      "ds_read_b128 v[180:183], %[x3]\n"
      "ds_read_b64_tr_b16 v[188:189], %[wa0] offset:24576\n"
      "ds_read_b64_tr_b16 v[190:191], %[wa0] offset:25600\n"
      "s_waitcnt lgkmcnt(4)\n"
      "v_mfma_f32_32x32x16_bf16 v[80:95], v[176:179], v[164:167], v[80:95]\n"
      "ds_read_b64_tr_b16 v[192:193], %[wa1] offset:24576\n"
      "ds_read_b64_tr_b16 v[194:195], %[wa1] offset:25600\n"
      "ds_read_b128 v[184:187], %[x3] offset:8192\n"
      "s_waitcnt lgkmcnt(6)\n"
      "v_mfma_f32_32x32x16_bf16 v[96:111], v[172:175], v[168:171], v[96:111]\n"
      "v_mfma_f32_32x32x16_bf16 v[112:127], v[176:179], v[168:171], v[112:127]\n"
      "s_waitcnt vmcnt(0) lgkmcnt(0)\n"
      "s_barrier\n"
      "s_mov_b32 s46, 0xffff0000\n"
      "v_add_u32 %[x0], s46, %[x0]\n"
      "v_add_u32 %[wa0], s46, %[wa0]\n"
      "v_add_u32 %[wa1], s46, %[wa1]\n"
      "v_mfma_f32_32x32x16_bf16 v[64:79], v[188:191], v[180:183], v[64:79]\n"
      "ds_read_b128 v[164:167], %[x0]\n"
      "ds_read_b64_tr_b16 v[172:173], %[wa0] offset:0\n"
      "ds_read_b64_tr_b16 v[174:175], %[wa0] offset:1024\n"
      "v_mfma_f32_32x32x16_bf16 v[80:95], v[192:195], v[180:183], v[80:95]\n"
      "ds_read_b64_tr_b16 v[176:177], %[wa1] offset:0\n"
      "ds_read_b64_tr_b16 v[178:179], %[wa1] offset:1024\n"
      "ds_read_b128 v[168:171], %[x0] offset:8192\n"
      "v_mfma_f32_32x32x16_bf16 v[96:111], v[188:191], v[184:187], v[96:111]\n"
      "v_mfma_f32_32x32x16_bf16 v[112:127], v[192:195], v[184:187], v[112:127]\n"
      "s_waitcnt lgkmcnt(3)\n"
      "v_add_u32 %[x1], s46, %[x1]\n"
      "v_mfma_f32_32x32x16_bf16 v[64:79], v[172:175], v[164:167], v[64:79]\n"
      "ds_read_b128 v[180:183], %[x1]\n"
      "ds_read_b64_tr_b16 v[188:189], %[wa0] offset:8192\n"
      "ds_read_b64_tr_b16 v[190:191], %[wa0] offset:9216\n"
      "s_waitcnt lgkmcnt(4)\n"
      "v_mfma_f32_32x32x16_bf16 v[80:95], v[176:179], v[164:167], v[80:95]\n"
      "ds_read_b64_tr_b16 v[192:193], %[wa1] offset:8192\n"
      "ds_read_b64_tr_b16 v[194:195], %[wa1] offset:9216\n"
      "ds_read_b128 v[184:187], %[x1] offset:8192\n"
      "s_waitcnt lgkmcnt(6)\n"
      "v_mfma_f32_32x32x16_bf16 v[96:111], v[172:175], v[168:171], v[96:111]\n"
      "v_mfma_f32_32x32x16_bf16 v[112:127], v[176:179], v[168:171], v[112:127]\n"
      "s_waitcnt lgkmcnt(3)\n"
      "v_add_u32 %[x2], s46, %[x2]\n"
      "v_mfma_f32_32x32x16_bf16 v[64:79], v[188:191], v[180:183], v[64:79]\n"
      "ds_read_b128 v[164:167], %[x2]\n"
      "ds_read_b64_tr_b16 v[172:173], %[wa0] offset:16384\n"
      "ds_read_b64_tr_b16 v[174:175], %[wa0] offset:17408\n"
      "s_waitcnt lgkmcnt(4)\n"
      "v_mfma_f32_32x32x16_bf16 v[80:95], v[192:195], v[180:183], v[80:95]\n"
      "ds_read_b64_tr_b16 v[176:177], %[wa1] offset:16384\n"
      "ds_read_b64_tr_b16 v[178:179], %[wa1] offset:17408\n"
      "ds_read_b128 v[168:171], %[x2] offset:8192\n"
      "s_waitcnt lgkmcnt(6)\n"
      "v_mfma_f32_32x32x16_bf16 v[96:111], v[188:191], v[184:187], v[96:111]\n"
      "v_mfma_f32_32x32x16_bf16 v[112:127], v[192:195], v[184:187], v[112:127]\n"
      "s_waitcnt lgkmcnt(3)\n"
      "v_add_u32 %[x3], s46, %[x3]\n"
      "v_mfma_f32_32x32x16_bf16 v[64:79], v[172:175], v[164:167], v[64:79]\n"
      "ds_read_b128 v[180:183], %[x3]\n"
      "ds_read_b64_tr_b16 v[188:189], %[wa0] offset:24576\n"
      "ds_read_b64_tr_b16 v[190:191], %[wa0] offset:25600\n"
      "s_waitcnt lgkmcnt(4)\n"
      "v_mfma_f32_32x32x16_bf16 v[80:95], v[176:179], v[164:167], v[80:95]\n"
      "ds_read_b64_tr_b16 v[192:193], %[wa1] offset:24576\n"
      "ds_read_b64_tr_b16 v[194:195], %[wa1] offset:25600\n"
      "ds_read_b128 v[184:187], %[x3] offset:8192\n"
      "s_waitcnt lgkmcnt(6)\n"
      "v_mfma_f32_32x32x16_bf16 v[96:111], v[172:175], v[168:171], v[96:111]\n"
      "v_mfma_f32_32x32x16_bf16 v[112:127], v[176:179], v[168:171], v[112:127]\n"
      "s_waitcnt lgkmcnt(0)\n"
      "v_mfma_f32_32x32x16_bf16 v[64:79], v[188:191], v[180:183], v[64:79]\n"
      "v_mfma_f32_32x32x16_bf16 v[80:95], v[192:195], v[180:183], v[80:95]\n"
      "v_mfma_f32_32x32x16_bf16 v[96:111], v[188:191], v[184:187], v[96:111]\n"
      "v_mfma_f32_32x32x16_bf16 v[112:127], v[192:195], v[184:187], v[112:127]\n"
      "s_nop 15\n"
        QKV_OPS);
        if (isq) { c00 *= QSCALE; c01 *= QSCALE; c10 *= QSCALE; c11 *= QSCALE; }
#pragma unroll
        for (int b = 0; b < 2; ++b) {
          st16(ws, da[b], pk8(b ? c01 : c00, 0));
          st16(ws, da[b] + ta, pk8(b ? c11 : c10, 0));
          const u32x4 a1 = pk8(b ? c01 : c00, 8), b1 = pk8(b ? c11 : c10, 8);
          if (!rem[b]) { st16(ws, db[b], a1); st16(ws, db[b] + ta, b1); }
          if (rem[b]) {
            st8(ws, db[b], (u32x2){a1[0], a1[1]}); st8(ws, dc[b], (u32x2){a1[2], a1[3]});
            st8(ws, db[b] + tbb, (u32x2){b1[0], b1[1]}); st8(ws, dc[b] + tbb, (u32x2){b1[2], b1[3]});
          }
        }
      }
    } else {
      if (w < 3) { asm volatile("s_waitcnt vmcnt(0)" ::: "memory"); odd_tables(); }
      for (int i = w; i < 50; i += 4) dma16((const char*)wo + mt * 40 * 1280, i * 1024 + l16, i * 1024);
      if (w == 3) {
        const unsigned k = (lane & 3) ? 0u : 0x3f803f80u;
        for (int i = 0; i < 32; ++i)
          st16(ws, WS_KV + 2 * mt * KV_HEAD + (2 * i + h) * KV_PAIR + 2560 + ((lane >> 2) & 7) * V_PLANE + (20 + (lane & 3)) * 16, (u32x4){k, k, k, k});
      }
      later();
      asm volatile("s_waitcnt lgkmcnt(0)\n\ts_barrier\n\ts_waitcnt vmcnt(0)\n\ts_barrier" ::: "memory");
      const int q = (lane & 15) >> 2, pp = lane & 3, ps = ((pp & 1) << 1) | (pp >> 1);
      for (int ct = w; ct < 20; ct += 4) {
        lptr a = lds + q * 1280 + ct * 64 + ((lane >> 4) & 1) * 32 + ps * 8;
        const u32x4 a0 = cat8(tr_read(a + (4 * h) * 1280), tr_read(a + (4 * h + 8) * 1280));
        const u32x4 a1 = cat8(tr_read(a + (20 + 4 * h) * 1280), tr_read(a + (28 + 4 * h) * 1280));
        const u32x4 bb = cat8(tr_read(a + 16 * 1280), tr_read(a + 36 * 1280));
        st16(ws, WS_WF + (ct * 40 + 2 * mt) * 1024 + l16, a0);
        st16(ws, WS_WF + (ct * 40 + 2 * mt + 1) * 1024 + l16, a1);
        if (h == (mt & 1)) st16(ws, WS_WF + (ct * 40 + 32 + (mt >> 1)) * 1024 + l16, bb);
      }
#pragma unroll
      for (int i = 0; i < 9; ++i) BAR();
    }
  } else if (w == 7) {
    const int row0 = 32 * rb + 8 * (lane & 3), dc0 = 160 * xp + 8 * (lane >> 2), dc1 = 160 * xp + 128 + 8 * ((lane >> 2) & 3);
    u32x4 ui[16];
    auto get = [&]() {
#pragma unroll
      for (int i = 0; i < 16; ++i) ui[i] = *(GAS const u32x4*)(u + (row0 + (i & 7)) * DM + (i < 8 ? dc0 : dc1));
    };
    if (UL0 < 0) get();
    BAR();
    unsigned uo[5];
#pragma unroll
    for (int i = 0; i < 5; ++i) {
      const int sl = i * 64 + lane, bk = sl / 5;
      uo[i] = WS_UT + bk * UT_BLK + min(sl - 5 * bk, 3) * 16;
      asm volatile("" : "+v"(uo[i]));
    }
    auto put = [&](int c) {
      u32x4 v;
#pragma unroll
      for (int j = 0; j < 4; ++j)
        v[j] = __builtin_amdgcn_perm(ui[(c & 8) + 2 * j + 1][(c & 7) >> 1], ui[(c & 8) + 2 * j][(c & 7) >> 1], (c & 1) ? 0x07060302u : 0x05040100u);
      if (c < 8 || lane < 16) st16u(ws, WS_UT + rb * UT_BLK + ((c < 8 ? dc0 : dc1) + (c & 7)) * 64 + (lane & 3) * 16, v);
    };
    auto put_all = [&]() {
#pragma unroll
      for (int c = 0; c < 16; ++c) asm volatile("" : "+v"(ui[c]));
#pragma unroll
      for (int c = 0; c < 16; ++c) put(c);
    };
    later();
    if (US0 == -2) put_all();
    BAR();
    const u64 tg = *(LAS u64*)(lds + L_MISC);
    if (US0 == -1) put_all();
    BAR();
    if (UL0 == 0) get();
    if (US0 == 0) put_all();
    unsigned todo = 1;
    u64 fl;
    bool late = false;
    auto fetch_u = [&]() {
#pragma unroll 1
      for (int c = 0; c < 3; ++c)
#pragma unroll
        for (int i = 0; i < 5; ++i)
          dma16(ws + (c == 2 ? d5 + 2 : d5 + 3 * odd + c) * 64, uo[i], L_U + c * U_SZ + U_PAD * U_BLK + i * 1024);
    };
    auto zero_pads = [&]() {
#pragma unroll
      for (int c = 0; c < 3; ++c) {
        lptr z = lds + L_U + c * U_SZ + l16;
        const u32x4 z4 = {0u, 0u, 0u, 0u};
        *(LAS u32x4*)z = z4; *(LAS u32x4*)(z + 1024) = z4; *(LAS u32x4*)(z + U_PAD * U_BLK - 1024) = z4;
      }
    };
    auto try_pub = [&](unsigned force) {
      asm volatile("s_cmp_eq_u32 %0, 0\n\ts_cbranch_scc1 1f\n\ts_getreg_b32 s44, hwreg(HW_REG_IB_STS)\n\ts_and_b32 s44, s44, %1\n\t"
                   "s_cbranch_scc1 1f\n\ts_waitcnt vmcnt(0)\n\ts_mov_b32 %0, 0\n\ts_mov_b64 exec, 1\n\tv_mov_b32 v64, %4\n\t"
                   "global_store_dwordx2 v64, %2, %3 sc1\n\ts_mov_b64 exec, -1\n1:"
                   : "+s"(todo) : "s"(force ? 0u : 0xc0000fu), "v"(tg), "s"(ws), "s"(WS_F0 + (xp * 64 + rb) * 8) : "memory", "scc", "s44", "v64");
    };
#pragma unroll
    for (int i = 1; i < 10; ++i) {
      BAR();
      if (i == UL0) get();
      if (i == US0) {
        put_all();
      } else if (i > US0) {
        try_pub(i == 9);
      }
      if (i == 5) zero_pads();
      if (i == 7) fl = __hip_atomic_load((GAS const u64*)(ws + WS_F0 + xp * 512 + (l16 >> 1)), __ATOMIC_RELAXED, __HIP_MEMORY_SCOPE_AGENT);
      if (i == UB) {
        if (!__all(fl == tg)) late = true; else fetch_u();
      }
    }
    if (late) { wait_flags(ws, WS_F0 + xp * 512 + (l16 >> 1), tg); fetch_u(); }
    if (blockIdx.x == 0) {
      const char* fp = ws + WS_F0 + (l16 >> 1);
#pragma unroll 1
      for (unsigned n = 0; n < SPIN_CAP; ++n) {
        const u64 a = __hip_atomic_load((GAS const u64*)(fp + 512), __ATOMIC_RELAXED, __HIP_MEMORY_SCOPE_AGENT),
                  b = __hip_atomic_load((GAS const u64*)(fp + 1024), __ATOMIC_RELAXED, __HIP_MEMORY_SCOPE_AGENT),
                  c = __hip_atomic_load((GAS const u64*)(fp + 1536), __ATOMIC_RELAXED, __HIP_MEMORY_SCOPE_AGENT);
        if (__all(a == tg && b == tg && c == tg)) break;
      }
      if (l16 == 0) st8(ws, WS_FL, __builtin_bit_cast(u32x2, tg));
    } else {
      asm volatile("s_waitcnt vmcnt(0)" ::: "memory");
    }
  } else {
    const unsigned la = *(GAS const unsigned*)(logA + dk * 128 + 2 * lane), ai = *(GAS const unsigned*)(Aim + dk * 128 + 2 * lane),
                   cr = *(GAS const unsigned*)(Cre + dk * 128 + 2 * lane), ci = *(GAS const unsigned*)(Cim + dk * 128 + 2 * lane),
                   ld = *(GAS const uint16_t*)(logdt + dk);
    const u64 tg = __hip_atomic_load((GAS const u64*)(ws + WS_FL), __ATOMIC_RELAXED, __HIP_MEMORY_SCOPE_AGENT) + 1;
    BAR();
    lptr tb = lds + L_TEVEN + (w - 4) * TAB_SZ;
    const int hq = (r >> 2) & 1, iq = (r & 3) + 4 * (r >> 3);
    const int trow = iq + (iq < 7 ? 4 * hq : iq < 11 ? 4 + 2 * hq : iq < 13 ? 6 + 2 * hq : iq < 15 ? iq - 5 + hq : 10);
    unsigned ea = trow * TROW + 16 * h, fp = (26 + r) * TROW + 16 * h;
    asm volatile("" : "+v"(ea), "+v"(fp));
    if (w == 4) { *(LAS u64*)(lds + L_MISC) = tg; *(LAS unsigned*)(lds + L_MISC + 8) = 0; }
    const float dt = rnd_bf(expf(bf_lo(ld)));
    const int ex = c_exp(cr, ci);
    gen_tables(tb + 4 * lane, bf_lo(la), bf_lo(ai), bf_lo(cr), bf_lo(ci), dt, ex);
    BAR_LDS();
    f16x8 fra[16], frb[16];
    lptr to = lds + L_TODD + (w - 4) * TAB_SZ;
#pragma unroll
    for (int s = 0; s < 8; ++s) { fra[s] = *(LAS f16x8*)(to + ea + 32 * s); frb[s] = *(LAS f16x8*)(to + fp + 32 * s); }
    BAR_LDS();
    if (gemm) {
      const int g0 = w == 6 ? 5 : 3 * (w - 4);
      asm volatile(
      "s_mov_b64 s[40:41], %[xs]\n"
      "s_mov_b64 s[42:43], %[wr]\n"
      "s_add_u32 s44, %[m0], 0\n"
      "s_mov_b32 m0, s44\n"
      "s_nop 0\n"
      "global_load_lds_dwordx4 %[p0], s[40:41] offset:0\n"
      "global_load_lds_dwordx4 %[p1], s[40:41] offset:1024\n"
      "global_load_lds_dwordx4 %[p2], s[42:43] offset:2048\n"
      "global_load_lds_dwordx4 %[p3], s[42:43] offset:3072\n"
      "s_add_u32 s40, s40, 20480\n"
      "s_addc_u32 s41, s41, 0\n"
      "s_add_u32 s42, s42, 30720\n"
      "s_addc_u32 s43, s43, 0\n"
      "s_add_u32 s44, %[m0], 4096\n"
      "s_mov_b32 m0, s44\n"
      "s_nop 0\n"
      "global_load_lds_dwordx4 %[p0], s[40:41] offset:0\n"
      "global_load_lds_dwordx4 %[p1], s[40:41] offset:1024\n"
      "global_load_lds_dwordx4 %[p2], s[42:43] offset:2048\n"
      "global_load_lds_dwordx4 %[p3], s[42:43] offset:3072\n"
      "s_add_u32 s40, s40, 20480\n"
      "s_addc_u32 s41, s41, 0\n"
      "s_add_u32 s42, s42, 30720\n"
      "s_addc_u32 s43, s43, 0\n"
      "s_add_u32 s44, %[m0], 8192\n"
      "s_mov_b32 m0, s44\n"
      "s_nop 0\n"
      "global_load_lds_dwordx4 %[p0], s[40:41] offset:0\n"
      "global_load_lds_dwordx4 %[p1], s[40:41] offset:1024\n"
      "global_load_lds_dwordx4 %[p2], s[42:43] offset:2048\n"
      "global_load_lds_dwordx4 %[p3], s[42:43] offset:3072\n"
        :
        : [p0] "v"(p0), [p1] "v"(p1), [p2] "v"(p2), [p3] "v"(p3), [xs] "s"(xb + kt2 * 128 + g0 * 20480),
          [wr] "s"(wb + kt2 * 245760ull + g0 * 30720), [m0] "s"(65536 + g0 * 4096)
        : "memory", "scc", "s40", "s41", "s42", "s43", "s44");
    }
#pragma unroll
    for (int s = 0; s < 8; ++s) { fra[8 + s] = *(LAS f16x8*)(tb + ea + 32 * s); frb[8 + s] = *(LAS f16x8*)(tb + fp + 32 * s); }
    f32x16 kp = {0};
#pragma unroll
    for (int s = 0; s < 16; ++s) kp = __builtin_amdgcn_mfma_f32_32x32x16_f16(fra[s], frb[s], kp, 0, 0, 0);
    const float kmul = ldexpf(2.0f * dt, ex);
#pragma unroll
    for (int i = 0; i < 16; ++i) kp[i] *= kmul;
    asm volatile("s_barrier" : "+v"(kp) :: "memory");
    later();
    asm volatile("s_waitcnt vmcnt(0)\n\ts_barrier" ::: "memory");
    BAR(); BAR(); BAR(); BAR(); BAR(); BAR();
    BAR();
    const bool last = lane == 63;
    const float n11 = next_lane(kp[11], lane0(kp[13])), n13 = next_lane(kp[13], lane0(kp[14])), n14 = next_lane(kp[14], lane0(kp[15]));
    lptr kc = lds + L_KC + (w - 4) * KC_SZ, zt = lds + L_Z + (w - 4) * 512;
    put_run(kc, 8 * r + 4 * h, kp[0], kp[1], kp[2], kp[3], kp[4], last ? kp[4] : kp[5], kp[6], true, true);
    put_run(kc, 256 + 8 * r + 4 * h, kp[7], kp[7], kp[8], kp[9], kp[9], kp[9], last ? kp[9] : kp[10], true, true);
    put_run(kc, 512 + 8 * lane, kp[11], kp[11], kp[11], kp[12], kp[12], kp[12], n11, true, true);
    put_run(kc, 516 + 8 * lane, kp[12], kp[12], n11, n11, n11, n11, n11, true, true);
    if (lane == 0) {
      put_run(kc, 1024, kp[13], kp[13], kp[13], kp[13], kp[13], 0.f, 0.f, true, true);
      put_run(kc, 1028, kp[13], 0.f, 0.f, 0.f, 0.f, 0.f, 0.f, true, false);
      put_run(kc, -4, 0.f, 0.f, 0.f, 0.f, kp[0], kp[1], kp[2], false, true);
    }
    if (lane < 56) *(LAS u32x2*)(kc + (lane & 3) * KC_COPY + 8 * (lane >> 2)) = (u32x2){0u, 0u};
    if (lane < 32) *(LAS u32x2*)(kc + (lane & 3) * KC_COPY + 2 * 1088 + 8 * (lane >> 2)) = (u32x2){0u, 0u};
    *(LAS unsigned*)(zt + 4 * lane) = 0u;
    *(LAS __bf16*)(zt + 256 + 2 * lane) = (__bf16)n13;
    *(LAS __bf16*)(zt + 384 + 2 * lane) = (__bf16)n14;
  }
  const u64 tag = *(LAS u64*)(lds + L_MISC);

  unsigned g = m0b + l16;
  f32x16 wr0, wr1, wr2, wr3, wr4, wr5;
  f32x4 wr6;
  if (w < 4) {
    asm volatile(
      "s_mov_b32 s45, 1\n"
      "v_mov_b32 v137, 160776\n"
      "v_mov_b32 v138, -1\n"
      "s_waitcnt lgkmcnt(0)\n"
      "s_barrier\n"
      "ds_read_b64 v[100:101], %[ka] offset:2112\n"
      "ds_read_b64 v[102:103], %[ka] offset:2120\n"
      "ds_read_b128 v[104:107], %[ua] offset:2640\n"
      "ds_read_b64 v[108:109], %[ka] offset:2144\n"
      "ds_read_b64 v[110:111], %[ka] offset:2152\n"
      "ds_read_b128 v[112:115], %[ua] offset:2672\n"
      "ds_read_b64 v[116:117], %[ka] offset:2048\n"
      "ds_read_b64 v[118:119], %[ka] offset:2056\n"
      "ds_read_b128 v[120:123], %[ua] offset:2560\n"
      "ds_read_b64 v[124:125], %[ka] offset:2080\n"
      "ds_read_b64 v[126:127], %[ka] offset:2088\n"
      "ds_read_b128 v[128:131], %[ua] offset:2592\n"
      "s_cmp_eq_u32 s45, 0\n"
      "s_cbranch_scc1 2f\n"
      "s_getreg_b32 s44, hwreg(HW_REG_IB_STS)\n"
      "s_and_b32 s44, s44, 0xc0000f\n"
      "s_cbranch_scc1 2f\n"
      "s_waitcnt vmcnt(0)\n"
      "s_mov_b32 s45, 0\n"
      "s_mov_b64 exec, 1\n"
      "ds_inc_rtn_u32 v136, v137, v138\n"
      "s_waitcnt lgkmcnt(0)\n"
      "v_cmp_eq_u32 vcc, 3, v136\n"
      "s_nop 1\n"
      "s_and_b64 exec, exec, vcc\n"
      "s_cbranch_execz 3f\n"
      "v_mov_b32 v136, %[f1]\n"
      "global_store_dwordx2 v136, %[tag], %[ws] sc1\n"
      "3:\n"
      "s_mov_b64 exec, -1\n"
      "2:\n"
      "s_waitcnt lgkmcnt(9)\n"
      "v_mfma_f32_32x32x16_bf16 %[y], v[100:103], v[104:107], 0\n"
      "ds_read_b64 v[100:101], %[ka] offset:1984\n"
      "ds_read_b64 v[102:103], %[ka] offset:1992\n"
      "ds_read_b128 v[104:107], %[ua] offset:2480\n"
      "s_waitcnt lgkmcnt(9)\n"
      "v_mfma_f32_32x32x16_bf16 %[y], v[108:111], v[112:115], %[y]\n"
      "ds_read_b64 v[108:109], %[ka] offset:2016\n"
      "ds_read_b64 v[110:111], %[ka] offset:2024\n"
      "ds_read_b128 v[112:115], %[ua] offset:2512\n"
      "s_waitcnt lgkmcnt(9)\n"
      "v_mfma_f32_32x32x16_bf16 %[y], v[116:119], v[120:123], %[y]\n"
      "ds_read_b64 v[116:117], %[ka] offset:1920\n"
      "ds_read_b64 v[118:119], %[ka] offset:1928\n"
      "ds_read_b128 v[120:123], %[ua] offset:2400\n"
      "s_cmp_eq_u32 s45, 0\n"
      "s_cbranch_scc1 2f\n"
      "s_getreg_b32 s44, hwreg(HW_REG_IB_STS)\n"
      "s_and_b32 s44, s44, 0xc0000f\n"
      "s_cbranch_scc1 2f\n"
      "s_waitcnt vmcnt(0)\n"
      "s_mov_b32 s45, 0\n"
      "s_mov_b64 exec, 1\n"
      "ds_inc_rtn_u32 v136, v137, v138\n"
      "s_waitcnt lgkmcnt(0)\n"
      "v_cmp_eq_u32 vcc, 3, v136\n"
      "s_nop 1\n"
      "s_and_b64 exec, exec, vcc\n"
      "s_cbranch_execz 3f\n"
      "v_mov_b32 v136, %[f1]\n"
      "global_store_dwordx2 v136, %[tag], %[ws] sc1\n"
      "3:\n"
      "s_mov_b64 exec, -1\n"
      "2:\n"
      "s_waitcnt lgkmcnt(9)\n"
      "v_mfma_f32_32x32x16_bf16 %[y], v[124:127], v[128:131], %[y]\n"
      "ds_read_b64 v[124:125], %[ka] offset:1952\n"
      "ds_read_b64 v[126:127], %[ka] offset:1960\n"
      "ds_read_b128 v[128:131], %[ua] offset:2432\n"
      "s_waitcnt lgkmcnt(9)\n"
      "v_mfma_f32_32x32x16_bf16 %[y], v[100:103], v[104:107], %[y]\n"
      "ds_read_b64 v[100:101], %[ka] offset:1856\n"
      "ds_read_b64 v[102:103], %[ka] offset:1864\n"
      "ds_read_b128 v[104:107], %[ua] offset:2320\n"
      "s_waitcnt lgkmcnt(9)\n"
      "v_mfma_f32_32x32x16_bf16 %[y], v[108:111], v[112:115], %[y]\n"
      "ds_read_b64 v[108:109], %[ka] offset:1888\n"
      "ds_read_b64 v[110:111], %[ka] offset:1896\n"
      "ds_read_b128 v[112:115], %[ua] offset:2352\n"
      "s_cmp_eq_u32 s45, 0\n"
      "s_cbranch_scc1 2f\n"
      "s_getreg_b32 s44, hwreg(HW_REG_IB_STS)\n"
      "s_and_b32 s44, s44, 0xc0000f\n"
      "s_cbranch_scc1 2f\n"
      "s_waitcnt vmcnt(0)\n"
      "s_mov_b32 s45, 0\n"
      "s_mov_b64 exec, 1\n"
      "ds_inc_rtn_u32 v136, v137, v138\n"
      "s_waitcnt lgkmcnt(0)\n"
      "v_cmp_eq_u32 vcc, 3, v136\n"
      "s_nop 1\n"
      "s_and_b64 exec, exec, vcc\n"
      "s_cbranch_execz 3f\n"
      "v_mov_b32 v136, %[f1]\n"
      "global_store_dwordx2 v136, %[tag], %[ws] sc1\n"
      "3:\n"
      "s_mov_b64 exec, -1\n"
      "2:\n"
      "s_waitcnt lgkmcnt(9)\n"
      "v_mfma_f32_32x32x16_bf16 %[y], v[116:119], v[120:123], %[y]\n"
      "ds_read_b64 v[116:117], %[ka] offset:1792\n"
      "ds_read_b64 v[118:119], %[ka] offset:1800\n"
      "ds_read_b128 v[120:123], %[ua] offset:2240\n"
      "s_waitcnt lgkmcnt(9)\n"
      "v_mfma_f32_32x32x16_bf16 %[y], v[124:127], v[128:131], %[y]\n"
      "ds_read_b64 v[124:125], %[ka] offset:1824\n"
      "ds_read_b64 v[126:127], %[ka] offset:1832\n"
      "ds_read_b128 v[128:131], %[ua] offset:2272\n"
      "s_waitcnt lgkmcnt(9)\n"
      "v_mfma_f32_32x32x16_bf16 %[y], v[100:103], v[104:107], %[y]\n"
      "ds_read_b64 v[100:101], %[ka] offset:1728\n"
      "ds_read_b64 v[102:103], %[ka] offset:1736\n"
      "ds_read_b128 v[104:107], %[ua] offset:2160\n"
      "s_cmp_eq_u32 s45, 0\n"
      "s_cbranch_scc1 2f\n"
      "s_getreg_b32 s44, hwreg(HW_REG_IB_STS)\n"
      "s_and_b32 s44, s44, 0xc0000f\n"
      "s_cbranch_scc1 2f\n"
      "s_waitcnt vmcnt(0)\n"
      "s_mov_b32 s45, 0\n"
      "s_mov_b64 exec, 1\n"
      "ds_inc_rtn_u32 v136, v137, v138\n"
      "s_waitcnt lgkmcnt(0)\n"
      "v_cmp_eq_u32 vcc, 3, v136\n"
      "s_nop 1\n"
      "s_and_b64 exec, exec, vcc\n"
      "s_cbranch_execz 3f\n"
      "v_mov_b32 v136, %[f1]\n"
      "global_store_dwordx2 v136, %[tag], %[ws] sc1\n"
      "3:\n"
      "s_mov_b64 exec, -1\n"
      "2:\n"
      "s_waitcnt lgkmcnt(9)\n"
      "v_mfma_f32_32x32x16_bf16 %[y], v[108:111], v[112:115], %[y]\n"
      "ds_read_b64 v[108:109], %[ka] offset:1760\n"
      "ds_read_b64 v[110:111], %[ka] offset:1768\n"
      "ds_read_b128 v[112:115], %[ua] offset:2192\n"
      "s_waitcnt lgkmcnt(9)\n"
      "v_mfma_f32_32x32x16_bf16 %[y], v[116:119], v[120:123], %[y]\n"
      "ds_read_b64 v[116:117], %[ka] offset:1664\n"
      "ds_read_b64 v[118:119], %[ka] offset:1672\n"
      "ds_read_b128 v[120:123], %[ua] offset:2080\n"
      "s_waitcnt lgkmcnt(9)\n"
      "v_mfma_f32_32x32x16_bf16 %[y], v[124:127], v[128:131], %[y]\n"
      "ds_read_b64 v[124:125], %[ka] offset:1696\n"
      "ds_read_b64 v[126:127], %[ka] offset:1704\n"
      "ds_read_b128 v[128:131], %[ua] offset:2112\n"
      "s_cmp_eq_u32 s45, 0\n"
      "s_cbranch_scc1 2f\n"
      "s_getreg_b32 s44, hwreg(HW_REG_IB_STS)\n"
      "s_and_b32 s44, s44, 0xc0000f\n"
      "s_cbranch_scc1 2f\n"
      "s_waitcnt vmcnt(0)\n"
      "s_mov_b32 s45, 0\n"
      "s_mov_b64 exec, 1\n"
      "ds_inc_rtn_u32 v136, v137, v138\n"
      "s_waitcnt lgkmcnt(0)\n"
      "v_cmp_eq_u32 vcc, 3, v136\n"
      "s_nop 1\n"
      "s_and_b64 exec, exec, vcc\n"
      "s_cbranch_execz 3f\n"
      "v_mov_b32 v136, %[f1]\n"
      "global_store_dwordx2 v136, %[tag], %[ws] sc1\n"
      "3:\n"
      "s_mov_b64 exec, -1\n"
      "2:\n"
      "s_waitcnt lgkmcnt(9)\n"
      "v_mfma_f32_32x32x16_bf16 %[y], v[100:103], v[104:107], %[y]\n"
      "ds_read_b64 v[100:101], %[ka] offset:1600\n"
      "ds_read_b64 v[102:103], %[ka] offset:1608\n"
      "ds_read_b128 v[104:107], %[ua] offset:2000\n"
      "s_waitcnt lgkmcnt(9)\n"
      "v_mfma_f32_32x32x16_bf16 %[y], v[108:111], v[112:115], %[y]\n"
      "ds_read_b64 v[108:109], %[ka] offset:1632\n"
      "ds_read_b64 v[110:111], %[ka] offset:1640\n"
      "ds_read_b128 v[112:115], %[ua] offset:2032\n"
      "s_waitcnt lgkmcnt(9)\n"
      "v_mfma_f32_32x32x16_bf16 %[y], v[116:119], v[120:123], %[y]\n"
      "ds_read_b64 v[116:117], %[ka] offset:1536\n"
      "ds_read_b64 v[118:119], %[ka] offset:1544\n"
      "ds_read_b128 v[120:123], %[ua] offset:1920\n"
      "s_cmp_eq_u32 s45, 0\n"
      "s_cbranch_scc1 2f\n"
      "s_getreg_b32 s44, hwreg(HW_REG_IB_STS)\n"
      "s_and_b32 s44, s44, 0xc0000f\n"
      "s_cbranch_scc1 2f\n"
      "s_waitcnt vmcnt(0)\n"
      "s_mov_b32 s45, 0\n"
      "s_mov_b64 exec, 1\n"
      "ds_inc_rtn_u32 v136, v137, v138\n"
      "s_waitcnt lgkmcnt(0)\n"
      "v_cmp_eq_u32 vcc, 3, v136\n"
      "s_nop 1\n"
      "s_and_b64 exec, exec, vcc\n"
      "s_cbranch_execz 3f\n"
      "v_mov_b32 v136, %[f1]\n"
      "global_store_dwordx2 v136, %[tag], %[ws] sc1\n"
      "3:\n"
      "s_mov_b64 exec, -1\n"
      "2:\n"
      "s_waitcnt lgkmcnt(9)\n"
      "v_mfma_f32_32x32x16_bf16 %[y], v[124:127], v[128:131], %[y]\n"
      "ds_read_b64 v[124:125], %[ka] offset:1568\n"
      "ds_read_b64 v[126:127], %[ka] offset:1576\n"
      "ds_read_b128 v[128:131], %[ua] offset:1952\n"
      "s_waitcnt lgkmcnt(9)\n"
      "v_mfma_f32_32x32x16_bf16 %[y], v[100:103], v[104:107], %[y]\n"
      "ds_read_b64 v[100:101], %[ka] offset:1472\n"
      "ds_read_b64 v[102:103], %[ka] offset:1480\n"
      "ds_read_b128 v[104:107], %[ua] offset:1840\n"
      "s_waitcnt lgkmcnt(9)\n"
      "v_mfma_f32_32x32x16_bf16 %[y], v[108:111], v[112:115], %[y]\n"
      "ds_read_b64 v[108:109], %[ka] offset:1504\n"
      "ds_read_b64 v[110:111], %[ka] offset:1512\n"
      "ds_read_b128 v[112:115], %[ua] offset:1872\n"
      "s_cmp_eq_u32 s45, 0\n"
      "s_cbranch_scc1 2f\n"
      "s_getreg_b32 s44, hwreg(HW_REG_IB_STS)\n"
      "s_and_b32 s44, s44, 0xc0000f\n"
      "s_cbranch_scc1 2f\n"
      "s_waitcnt vmcnt(0)\n"
      "s_mov_b32 s45, 0\n"
      "s_mov_b64 exec, 1\n"
      "ds_inc_rtn_u32 v136, v137, v138\n"
      "s_waitcnt lgkmcnt(0)\n"
      "v_cmp_eq_u32 vcc, 3, v136\n"
      "s_nop 1\n"
      "s_and_b64 exec, exec, vcc\n"
      "s_cbranch_execz 3f\n"
      "v_mov_b32 v136, %[f1]\n"
      "global_store_dwordx2 v136, %[tag], %[ws] sc1\n"
      "3:\n"
      "s_mov_b64 exec, -1\n"
      "2:\n"
      "s_waitcnt lgkmcnt(9)\n"
      "v_mfma_f32_32x32x16_bf16 %[y], v[116:119], v[120:123], %[y]\n"
      "ds_read_b64 v[116:117], %[ka] offset:1408\n"
      "ds_read_b64 v[118:119], %[ka] offset:1416\n"
      "ds_read_b128 v[120:123], %[ua] offset:1760\n"
      "s_waitcnt lgkmcnt(9)\n"
      "v_mfma_f32_32x32x16_bf16 %[y], v[124:127], v[128:131], %[y]\n"
      "ds_read_b64 v[124:125], %[ka] offset:1440\n"
      "ds_read_b64 v[126:127], %[ka] offset:1448\n"
      "ds_read_b128 v[128:131], %[ua] offset:1792\n"
      "s_waitcnt lgkmcnt(9)\n"
      "v_mfma_f32_32x32x16_bf16 %[y], v[100:103], v[104:107], %[y]\n"
      "ds_read_b64 v[100:101], %[ka] offset:1344\n"
      "ds_read_b64 v[102:103], %[ka] offset:1352\n"
      "ds_read_b128 v[104:107], %[ua] offset:1680\n"
      "s_cmp_eq_u32 s45, 0\n"
      "s_cbranch_scc1 2f\n"
      "s_getreg_b32 s44, hwreg(HW_REG_IB_STS)\n"
      "s_and_b32 s44, s44, 0xc0000f\n"
      "s_cbranch_scc1 2f\n"
      "s_waitcnt vmcnt(0)\n"
      "s_mov_b32 s45, 0\n"
      "s_mov_b64 exec, 1\n"
      "ds_inc_rtn_u32 v136, v137, v138\n"
      "s_waitcnt lgkmcnt(0)\n"
      "v_cmp_eq_u32 vcc, 3, v136\n"
      "s_nop 1\n"
      "s_and_b64 exec, exec, vcc\n"
      "s_cbranch_execz 3f\n"
      "v_mov_b32 v136, %[f1]\n"
      "global_store_dwordx2 v136, %[tag], %[ws] sc1\n"
      "3:\n"
      "s_mov_b64 exec, -1\n"
      "2:\n"
      "s_waitcnt lgkmcnt(9)\n"
      "v_mfma_f32_32x32x16_bf16 %[y], v[108:111], v[112:115], %[y]\n"
      "ds_read_b64 v[108:109], %[ka] offset:1376\n"
      "ds_read_b64 v[110:111], %[ka] offset:1384\n"
      "ds_read_b128 v[112:115], %[ua] offset:1712\n"
      "s_waitcnt lgkmcnt(9)\n"
      "v_mfma_f32_32x32x16_bf16 %[y], v[116:119], v[120:123], %[y]\n"
      "ds_read_b64 v[116:117], %[ka] offset:1280\n"
      "ds_read_b64 v[118:119], %[ka] offset:1288\n"
      "ds_read_b128 v[120:123], %[ua] offset:1600\n"
      "s_waitcnt lgkmcnt(9)\n"
      "v_mfma_f32_32x32x16_bf16 %[y], v[124:127], v[128:131], %[y]\n"
      "ds_read_b64 v[124:125], %[ka] offset:1312\n"
      "ds_read_b64 v[126:127], %[ka] offset:1320\n"
      "ds_read_b128 v[128:131], %[ua] offset:1632\n"
      "s_cmp_eq_u32 s45, 0\n"
      "s_cbranch_scc1 2f\n"
      "s_getreg_b32 s44, hwreg(HW_REG_IB_STS)\n"
      "s_and_b32 s44, s44, 0xc0000f\n"
      "s_cbranch_scc1 2f\n"
      "s_waitcnt vmcnt(0)\n"
      "s_mov_b32 s45, 0\n"
      "s_mov_b64 exec, 1\n"
      "ds_inc_rtn_u32 v136, v137, v138\n"
      "s_waitcnt lgkmcnt(0)\n"
      "v_cmp_eq_u32 vcc, 3, v136\n"
      "s_nop 1\n"
      "s_and_b64 exec, exec, vcc\n"
      "s_cbranch_execz 3f\n"
      "v_mov_b32 v136, %[f1]\n"
      "global_store_dwordx2 v136, %[tag], %[ws] sc1\n"
      "3:\n"
      "s_mov_b64 exec, -1\n"
      "2:\n"
      "s_waitcnt lgkmcnt(9)\n"
      "v_mfma_f32_32x32x16_bf16 %[y], v[100:103], v[104:107], %[y]\n"
      "ds_read_b64 v[100:101], %[ka] offset:1216\n"
      "ds_read_b64 v[102:103], %[ka] offset:1224\n"
      "ds_read_b128 v[104:107], %[ua] offset:1520\n"
      "s_waitcnt lgkmcnt(9)\n"
      "v_mfma_f32_32x32x16_bf16 %[y], v[108:111], v[112:115], %[y]\n"
      "ds_read_b64 v[108:109], %[ka] offset:1248\n"
      "ds_read_b64 v[110:111], %[ka] offset:1256\n"
      "ds_read_b128 v[112:115], %[ua] offset:1552\n"
      "s_waitcnt lgkmcnt(9)\n"
      "v_mfma_f32_32x32x16_bf16 %[y], v[116:119], v[120:123], %[y]\n"
      "ds_read_b64 v[116:117], %[ka] offset:1152\n"
      "ds_read_b64 v[118:119], %[ka] offset:1160\n"
      "ds_read_b128 v[120:123], %[ua] offset:1440\n"
      "s_cmp_eq_u32 s45, 0\n"
      "s_cbranch_scc1 2f\n"
      "s_getreg_b32 s44, hwreg(HW_REG_IB_STS)\n"
      "s_and_b32 s44, s44, 0xc0000f\n"
      "s_cbranch_scc1 2f\n"
      "s_waitcnt vmcnt(0)\n"
      "s_mov_b32 s45, 0\n"
      "s_mov_b64 exec, 1\n"
      "ds_inc_rtn_u32 v136, v137, v138\n"
      "s_waitcnt lgkmcnt(0)\n"
      "v_cmp_eq_u32 vcc, 3, v136\n"
      "s_nop 1\n"
      "s_and_b64 exec, exec, vcc\n"
      "s_cbranch_execz 3f\n"
      "v_mov_b32 v136, %[f1]\n"
      "global_store_dwordx2 v136, %[tag], %[ws] sc1\n"
      "3:\n"
      "s_mov_b64 exec, -1\n"
      "2:\n"
      "s_waitcnt lgkmcnt(9)\n"
      "v_mfma_f32_32x32x16_bf16 %[y], v[124:127], v[128:131], %[y]\n"
      "ds_read_b64 v[124:125], %[ka] offset:1184\n"
      "ds_read_b64 v[126:127], %[ka] offset:1192\n"
      "ds_read_b128 v[128:131], %[ua] offset:1472\n"
      "s_waitcnt lgkmcnt(9)\n"
      "v_mfma_f32_32x32x16_bf16 %[y], v[100:103], v[104:107], %[y]\n"
      "ds_read_b64 v[100:101], %[ka] offset:1088\n"
      "ds_read_b64 v[102:103], %[ka] offset:1096\n"
      "ds_read_b128 v[104:107], %[ua] offset:1360\n"
      "s_waitcnt lgkmcnt(9)\n"
      "v_mfma_f32_32x32x16_bf16 %[y], v[108:111], v[112:115], %[y]\n"
      "ds_read_b64 v[108:109], %[ka] offset:1120\n"
      "ds_read_b64 v[110:111], %[ka] offset:1128\n"
      "ds_read_b128 v[112:115], %[ua] offset:1392\n"
      "s_cmp_eq_u32 s45, 0\n"
      "s_cbranch_scc1 2f\n"
      "s_waitcnt vmcnt(0)\n"
      "s_mov_b32 s45, 0\n"
      "s_mov_b64 exec, 1\n"
      "ds_inc_rtn_u32 v136, v137, v138\n"
      "s_waitcnt lgkmcnt(0)\n"
      "v_cmp_eq_u32 vcc, 3, v136\n"
      "s_nop 1\n"
      "s_and_b64 exec, exec, vcc\n"
      "s_cbranch_execz 3f\n"
      "v_mov_b32 v136, %[f1]\n"
      "global_store_dwordx2 v136, %[tag], %[ws] sc1\n"
      "3:\n"
      "s_mov_b64 exec, -1\n"
      "2:\n"
      "s_waitcnt lgkmcnt(9)\n"
      "v_mfma_f32_32x32x16_bf16 %[y], v[116:119], v[120:123], %[y]\n"
      "ds_read_b64 v[116:117], %[ka] offset:1024\n"
      "ds_read_b64 v[118:119], %[ka] offset:1032\n"
      "ds_read_b128 v[120:123], %[ua] offset:1280\n"
      "s_waitcnt lgkmcnt(9)\n"
      "v_mfma_f32_32x32x16_bf16 %[y], v[124:127], v[128:131], %[y]\n"
      "ds_read_b64 v[124:125], %[ka] offset:1056\n"
      "ds_read_b64 v[126:127], %[ka] offset:1064\n"
      "ds_read_b128 v[128:131], %[ua] offset:1312\n"
      "s_waitcnt lgkmcnt(9)\n"
      "v_mfma_f32_32x32x16_bf16 %[y], v[100:103], v[104:107], %[y]\n"
      "ds_read_b64 v[100:101], %[ka] offset:960\n"
      "ds_read_b64 v[102:103], %[ka] offset:968\n"
      "ds_read_b128 v[104:107], %[ua] offset:1200\n"
      "s_waitcnt lgkmcnt(9)\n"
      "v_mfma_f32_32x32x16_bf16 %[y], v[108:111], v[112:115], %[y]\n"
      "ds_read_b64 v[108:109], %[ka] offset:992\n"
      "ds_read_b64 v[110:111], %[ka] offset:1000\n"
      "ds_read_b128 v[112:115], %[ua] offset:1232\n"
      "s_waitcnt lgkmcnt(9)\n"
      "v_mfma_f32_32x32x16_bf16 %[y], v[116:119], v[120:123], %[y]\n"
      "ds_read_b64 v[116:117], %[ka] offset:896\n"
      "ds_read_b64 v[118:119], %[ka] offset:904\n"
      "ds_read_b128 v[120:123], %[ua] offset:1120\n"
      "s_waitcnt lgkmcnt(9)\n"
      "v_mfma_f32_32x32x16_bf16 %[y], v[124:127], v[128:131], %[y]\n"
      "ds_read_b64 v[124:125], %[ka] offset:928\n"
      "ds_read_b64 v[126:127], %[ka] offset:936\n"
      "ds_read_b128 v[128:131], %[ua] offset:1152\n"
      "s_waitcnt lgkmcnt(9)\n"
      "v_mfma_f32_32x32x16_bf16 %[y], v[100:103], v[104:107], %[y]\n"
      "ds_read_b64 v[100:101], %[ka] offset:832\n"
      "ds_read_b64 v[102:103], %[ka] offset:840\n"
      "ds_read_b128 v[104:107], %[ua] offset:1040\n"
      "s_waitcnt lgkmcnt(9)\n"
      "v_mfma_f32_32x32x16_bf16 %[y], v[108:111], v[112:115], %[y]\n"
      "ds_read_b64 v[108:109], %[ka] offset:864\n"
      "ds_read_b64 v[110:111], %[ka] offset:872\n"
      "ds_read_b128 v[112:115], %[ua] offset:1072\n"
      "s_waitcnt lgkmcnt(9)\n"
      "v_mfma_f32_32x32x16_bf16 %[y], v[116:119], v[120:123], %[y]\n"
      "ds_read_b64 v[116:117], %[ka] offset:768\n"
      "ds_read_b64 v[118:119], %[ka] offset:776\n"
      "ds_read_b128 v[120:123], %[ua] offset:960\n"
      "s_waitcnt lgkmcnt(9)\n"
      "v_mfma_f32_32x32x16_bf16 %[y], v[124:127], v[128:131], %[y]\n"
      "ds_read_b64 v[124:125], %[ka] offset:800\n"
      "ds_read_b64 v[126:127], %[ka] offset:808\n"
      "ds_read_b128 v[128:131], %[ua] offset:992\n"
      "s_waitcnt lgkmcnt(9)\n"
      "v_mfma_f32_32x32x16_bf16 %[y], v[100:103], v[104:107], %[y]\n"
      "ds_read_b64 v[100:101], %[ka] offset:704\n"
      "ds_read_b64 v[102:103], %[ka] offset:712\n"
      "ds_read_b128 v[104:107], %[ua] offset:880\n"
      "s_waitcnt lgkmcnt(9)\n"
      "v_mfma_f32_32x32x16_bf16 %[y], v[108:111], v[112:115], %[y]\n"
      "ds_read_b64 v[108:109], %[ka] offset:736\n"
      "ds_read_b64 v[110:111], %[ka] offset:744\n"
      "ds_read_b128 v[112:115], %[ua] offset:912\n"
      "s_waitcnt lgkmcnt(9)\n"
      "v_mfma_f32_32x32x16_bf16 %[y], v[116:119], v[120:123], %[y]\n"
      "ds_read_b64 v[116:117], %[ka] offset:640\n"
      "ds_read_b64 v[118:119], %[ka] offset:648\n"
      "ds_read_b128 v[120:123], %[ua] offset:800\n"
      "s_waitcnt lgkmcnt(9)\n"
      "v_mfma_f32_32x32x16_bf16 %[y], v[124:127], v[128:131], %[y]\n"
      "ds_read_b64 v[124:125], %[ka] offset:672\n"
      "ds_read_b64 v[126:127], %[ka] offset:680\n"
      "ds_read_b128 v[128:131], %[ua] offset:832\n"
      "global_load_dwordx4 v[136:139], %[po], %[ws] sc1\n"
      "s_waitcnt lgkmcnt(9)\n"
      "v_mfma_f32_32x32x16_bf16 %[y], v[100:103], v[104:107], %[y]\n"
      "ds_read_b64 v[100:101], %[ka] offset:576\n"
      "ds_read_b64 v[102:103], %[ka] offset:584\n"
      "ds_read_b128 v[104:107], %[ua] offset:720\n"
      "s_waitcnt lgkmcnt(9)\n"
      "v_mfma_f32_32x32x16_bf16 %[y], v[108:111], v[112:115], %[y]\n"
      "ds_read_b64 v[108:109], %[ka] offset:608\n"
      "ds_read_b64 v[110:111], %[ka] offset:616\n"
      "ds_read_b128 v[112:115], %[ua] offset:752\n"
      "s_waitcnt lgkmcnt(9)\n"
      "v_mfma_f32_32x32x16_bf16 %[y], v[116:119], v[120:123], %[y]\n"
      "ds_read_b64 v[116:117], %[ka] offset:512\n"
      "ds_read_b64 v[118:119], %[ka] offset:520\n"
      "ds_read_b128 v[120:123], %[ua] offset:640\n"
      "s_waitcnt lgkmcnt(9)\n"
      "v_mfma_f32_32x32x16_bf16 %[y], v[124:127], v[128:131], %[y]\n"
      "ds_read_b64 v[124:125], %[ka] offset:544\n"
      "ds_read_b64 v[126:127], %[ka] offset:552\n"
      "ds_read_b128 v[128:131], %[ua] offset:672\n"
      "s_waitcnt lgkmcnt(9)\n"
      "v_mfma_f32_32x32x16_bf16 %[y], v[100:103], v[104:107], %[y]\n"
      "ds_read_b64 v[100:101], %[ka] offset:448\n"
      "ds_read_b64 v[102:103], %[ka] offset:456\n"
      "ds_read_b128 v[104:107], %[ua] offset:560\n"
      "s_waitcnt lgkmcnt(9)\n"
      "v_mfma_f32_32x32x16_bf16 %[y], v[108:111], v[112:115], %[y]\n"
      "ds_read_b64 v[108:109], %[ka] offset:480\n"
      "ds_read_b64 v[110:111], %[ka] offset:488\n"
      "ds_read_b128 v[112:115], %[ua] offset:592\n"
      "s_waitcnt lgkmcnt(9)\n"
      "v_mfma_f32_32x32x16_bf16 %[y], v[116:119], v[120:123], %[y]\n"
      "ds_read_b64 v[116:117], %[ka] offset:384\n"
      "ds_read_b64 v[118:119], %[ka] offset:392\n"
      "ds_read_b128 v[120:123], %[ua] offset:480\n"
      "s_waitcnt lgkmcnt(9)\n"
      "v_mfma_f32_32x32x16_bf16 %[y], v[124:127], v[128:131], %[y]\n"
      "ds_read_b64 v[124:125], %[ka] offset:416\n"
      "ds_read_b64 v[126:127], %[ka] offset:424\n"
      "ds_read_b128 v[128:131], %[ua] offset:512\n"
      "s_waitcnt lgkmcnt(9)\n"
      "v_mfma_f32_32x32x16_bf16 %[y], v[100:103], v[104:107], %[y]\n"
      "ds_read_b64 v[100:101], %[ka] offset:320\n"
      "ds_read_b64 v[102:103], %[ka] offset:328\n"
      "ds_read_b128 v[104:107], %[ua] offset:400\n"
      "s_waitcnt lgkmcnt(9)\n"
      "v_mfma_f32_32x32x16_bf16 %[y], v[108:111], v[112:115], %[y]\n"
      "ds_read_b64 v[108:109], %[ka] offset:352\n"
      "ds_read_b64 v[110:111], %[ka] offset:360\n"
      "ds_read_b128 v[112:115], %[ua] offset:432\n"
      "s_waitcnt lgkmcnt(9)\n"
      "v_mfma_f32_32x32x16_bf16 %[y], v[116:119], v[120:123], %[y]\n"
      "ds_read_b64 v[116:117], %[ka] offset:256\n"
      "ds_read_b64 v[118:119], %[ka] offset:264\n"
      "ds_read_b128 v[120:123], %[ua] offset:320\n"
      "s_waitcnt lgkmcnt(9)\n"
      "v_mfma_f32_32x32x16_bf16 %[y], v[124:127], v[128:131], %[y]\n"
      "ds_read_b64 v[124:125], %[ka] offset:288\n"
      "ds_read_b64 v[126:127], %[ka] offset:296\n"
      "ds_read_b128 v[128:131], %[ua] offset:352\n"
      "s_waitcnt vmcnt(0)\n"
      "v_cmp_eq_u64 vcc, v[136:137], %[tag]\n"
      "v_cmp_eq_u64 s[46:47], v[138:139], %[tag]\n"
      "s_nop 1\n"
      "s_and_b64 vcc, vcc, s[46:47]\n"
      "s_cmp_eq_u64 vcc, -1\n"
      "s_cbranch_scc1 5f\n"
      "global_load_dwordx4 v[136:139], %[po], %[ws] sc1\n"
      "s_mov_b32 s45, 1\n"
      "s_branch 6f\n"
      "5:\n"
      "s_mov_b32 m0, %[qm]\n"
      "v_add_u32 v136, 0x100, %[l16]\n"
      "global_load_lds_dwordx4 %[l16], %[qt] sc1\n"
      "global_load_lds_dwordx4 v136, %[qt] offset:1024 sc1\n"
      "s_mov_b32 m0, %[qn]\n"
      "s_mov_b32 exec_hi, 0\n"
      "global_load_lds_dwordx4 %[qo], %[qt] sc1\n"
      "s_mov_b32 exec_hi, -1\n"
      "s_add_u32 s44, %[km], 0\n"
      "s_mov_b32 m0, s44\n"
      "v_add_u32 v136, s44, %[l16]\n"
      "global_load_lds_dwordx4 v136, %[kv] offset:0 sc1\n"
      "global_load_lds_dwordx4 v136, %[kv] offset:1024 sc1\n"
      "global_load_lds_dwordx4 v136, %[kv] offset:2048 sc1\n"
      "global_load_lds_dwordx4 v136, %[kv] offset:3072 sc1\n"
      "s_add_u32 s44, %[km], 4096\n"
      "s_mov_b32 m0, s44\n"
      "v_add_u32 v136, s44, %[l16]\n"
      "global_load_lds_dwordx4 v136, %[kv] offset:0 sc1\n"
      "global_load_lds_dwordx4 v136, %[kv] offset:1024 sc1\n"
      "global_load_lds_dwordx4 v136, %[kv] offset:2048 sc1\n"
      "global_load_lds_dwordx4 v136, %[kv] offset:3072 sc1\n"
      "s_add_u32 s44, %[km], 32768\n"
      "s_mov_b32 m0, s44\n"
      "v_add_u32 v136, s44, %[l16]\n"
      "global_load_lds_dwordx4 v136, %[kv] offset:0 sc1\n"
      "global_load_lds_dwordx4 v136, %[kv] offset:1024 sc1\n"
      "global_load_lds_dwordx4 v136, %[kv] offset:2048 sc1\n"
      "global_load_lds_dwordx4 v136, %[kv] offset:3072 sc1\n"
      "s_add_u32 s44, %[km], 36864\n"
      "s_mov_b32 m0, s44\n"
      "v_add_u32 v136, s44, %[l16]\n"
      "global_load_lds_dwordx4 v136, %[kv] offset:0 sc1\n"
      "global_load_lds_dwordx4 v136, %[kv] offset:1024 sc1\n"
      "global_load_lds_dwordx4 v136, %[kv] offset:2048 sc1\n"
      "global_load_lds_dwordx4 v136, %[kv] offset:3072 sc1\n"
      "6:\n"
      "s_waitcnt lgkmcnt(9)\n"
      "v_mfma_f32_32x32x16_bf16 %[y], v[100:103], v[104:107], %[y]\n"
      "ds_read_b64 v[100:101], %[ka] offset:192\n"
      "ds_read_b64 v[102:103], %[ka] offset:200\n"
      "ds_read_b128 v[104:107], %[ua] offset:240\n"
      "s_waitcnt lgkmcnt(9)\n"
      "v_mfma_f32_32x32x16_bf16 %[y], v[108:111], v[112:115], %[y]\n"
      "ds_read_b64 v[108:109], %[ka] offset:224\n"
      "ds_read_b64 v[110:111], %[ka] offset:232\n"
      "ds_read_b128 v[112:115], %[ua] offset:272\n"
      "s_waitcnt lgkmcnt(9)\n"
      "v_mfma_f32_32x32x16_bf16 %[y], v[116:119], v[120:123], %[y]\n"
      "ds_read_b64 v[116:117], %[ka] offset:128\n"
      "ds_read_b64 v[118:119], %[ka] offset:136\n"
      "ds_read_b128 v[120:123], %[ua] offset:160\n"
      "s_waitcnt lgkmcnt(9)\n"
      "v_mfma_f32_32x32x16_bf16 %[y], v[124:127], v[128:131], %[y]\n"
      "ds_read_b64 v[124:125], %[ka] offset:160\n"
      "ds_read_b64 v[126:127], %[ka] offset:168\n"
      "ds_read_b128 v[128:131], %[ua] offset:192\n"
      "s_waitcnt lgkmcnt(9)\n"
      "v_mfma_f32_32x32x16_bf16 %[y], v[100:103], v[104:107], %[y]\n"
      "ds_read_b64 v[100:101], %[ka] offset:64\n"
      "ds_read_b64 v[102:103], %[ka] offset:72\n"
      "ds_read_b128 v[104:107], %[ua] offset:80\n"
      "s_waitcnt lgkmcnt(9)\n"
      "v_mfma_f32_32x32x16_bf16 %[y], v[108:111], v[112:115], %[y]\n"
      "ds_read_b64 v[108:109], %[ka] offset:96\n"
      "ds_read_b64 v[110:111], %[ka] offset:104\n"
      "ds_read_b128 v[112:115], %[ua] offset:112\n"
      "s_waitcnt lgkmcnt(9)\n"
      "v_mfma_f32_32x32x16_bf16 %[y], v[116:119], v[120:123], %[y]\n"
      "ds_read_b64 v[116:117], %[ka] offset:0\n"
      "ds_read_b64 v[118:119], %[ka] offset:8\n"
      "ds_read_b128 v[120:123], %[ua] offset:0\n"
      "s_waitcnt lgkmcnt(9)\n"
      "v_mfma_f32_32x32x16_bf16 %[y], v[124:127], v[128:131], %[y]\n"
      "ds_read_b64 v[124:125], %[ka] offset:32\n"
      "ds_read_b64 v[126:127], %[ka] offset:40\n"
      "ds_read_b128 v[128:131], %[ua] offset:32\n"
      "s_waitcnt lgkmcnt(9)\n"
      "v_mfma_f32_32x32x16_bf16 %[y], v[100:103], v[104:107], %[y]\n"
      "s_waitcnt lgkmcnt(6)\n"
      "v_mfma_f32_32x32x16_bf16 %[y], v[108:111], v[112:115], %[y]\n"
      "s_waitcnt lgkmcnt(3)\n"
      "v_mfma_f32_32x32x16_bf16 %[y], v[116:119], v[120:123], %[y]\n"
      "s_waitcnt lgkmcnt(0)\n"
      "v_mfma_f32_32x32x16_bf16 %[y], v[124:127], v[128:131], %[y]\n"
      "s_cmp_eq_u32 s45, 0\n"
      "s_cbranch_scc1 7f\n"
      "s_mov_b32 s44, 0\n"
      "s_waitcnt vmcnt(0)\n"
      "3:\n"
      "v_cmp_eq_u64 vcc, v[136:137], %[tag]\n"
      "v_cmp_eq_u64 s[46:47], v[138:139], %[tag]\n"
      "s_nop 1\n"
      "s_and_b64 vcc, vcc, s[46:47]\n"
      "s_cmp_eq_u64 vcc, -1\n"
      "s_cbranch_scc1 4f\n"
      "s_add_u32 s44, s44, 1\n"
      "s_cmp_gt_u32 s44, 0x8000\n"
      "s_cbranch_scc1 4f\n"
      "s_sleep 1\n"
      "global_load_dwordx4 v[136:139], %[po], %[ws] sc1\n"
      "s_waitcnt vmcnt(0)\n"
      "s_branch 3b\n"
      "4:\n"
      "s_mov_b32 m0, %[qm]\n"
      "v_add_u32 v136, 0x100, %[l16]\n"
      "global_load_lds_dwordx4 %[l16], %[qt] sc1\n"
      "global_load_lds_dwordx4 v136, %[qt] offset:1024 sc1\n"
      "s_mov_b32 m0, %[qn]\n"
      "s_mov_b32 exec_hi, 0\n"
      "global_load_lds_dwordx4 %[qo], %[qt] sc1\n"
      "s_mov_b32 exec_hi, -1\n"
      "s_add_u32 s44, %[km], 0\n"
      "s_mov_b32 m0, s44\n"
      "v_add_u32 v136, s44, %[l16]\n"
      "global_load_lds_dwordx4 v136, %[kv] offset:0 sc1\n"
      "global_load_lds_dwordx4 v136, %[kv] offset:1024 sc1\n"
      "global_load_lds_dwordx4 v136, %[kv] offset:2048 sc1\n"
      "global_load_lds_dwordx4 v136, %[kv] offset:3072 sc1\n"
      "s_add_u32 s44, %[km], 4096\n"
      "s_mov_b32 m0, s44\n"
      "v_add_u32 v136, s44, %[l16]\n"
      "global_load_lds_dwordx4 v136, %[kv] offset:0 sc1\n"
      "global_load_lds_dwordx4 v136, %[kv] offset:1024 sc1\n"
      "global_load_lds_dwordx4 v136, %[kv] offset:2048 sc1\n"
      "global_load_lds_dwordx4 v136, %[kv] offset:3072 sc1\n"
      "s_add_u32 s44, %[km], 32768\n"
      "s_mov_b32 m0, s44\n"
      "v_add_u32 v136, s44, %[l16]\n"
      "global_load_lds_dwordx4 v136, %[kv] offset:0 sc1\n"
      "global_load_lds_dwordx4 v136, %[kv] offset:1024 sc1\n"
      "global_load_lds_dwordx4 v136, %[kv] offset:2048 sc1\n"
      "global_load_lds_dwordx4 v136, %[kv] offset:3072 sc1\n"
      "s_add_u32 s44, %[km], 36864\n"
      "s_mov_b32 m0, s44\n"
      "v_add_u32 v136, s44, %[l16]\n"
      "global_load_lds_dwordx4 v136, %[kv] offset:0 sc1\n"
      "global_load_lds_dwordx4 v136, %[kv] offset:1024 sc1\n"
      "global_load_lds_dwordx4 v136, %[kv] offset:2048 sc1\n"
      "global_load_lds_dwordx4 v136, %[kv] offset:3072 sc1\n"
      "7:\n"
      "s_nop 15\n"
      : [y] "=&{v[154:169]}"(wr0)
      : [ka] "{v20}"(ka), [ua] "{v21}"(ua), [f1] "s"(f1), [tag] "v"(tag), [ws] "s"(ws), [po] "{v23}"(po1), [l16] "v"(l16),
        [qt] "s"(qt), [kv] "s"(kv), [km] "s"(w * 8192), [qm] "s"(L_QA + w * 2048), [qn] "s"(L_QB + w * 512),
        [qo] "v"(1024 + ((l16 >> 8) & 1) * Q_TILE + (l16 & 240))
      : "memory", "scc", "vcc", "s44", "s45", "s46", "s47", V10(10), V10(11), V10(12), V10(13));
  } else {
    lptr bx = lds + L_BOX + (w - 4) * BOX_SZ;
    BAR_LDS();
    {
      if (w < 6 || (w == 6 && !odd)) {
        const int ln = l16 >> 4, c0 = 2 * ln - 2 + 4 * U_PAD, c1 = c0 + 2;
        lptr ub = lds + L_U + (w - 4) * U_SZ;
        lptr pa = ub + (c0 >> 2) * U_BLK + (c0 & 3) * 16, pb = ub + (c1 >> 2) * U_BLK + (c1 & 3) * 16;
        const u32x4 x0 = *(LAS u32x4*)pa, x1 = *(LAS u32x4*)(pa + 16), x2 = *(LAS u32x4*)pb, x3 = *(LAS u32x4*)(pb + 16);
        const unsigned xw[16] = {x0[0], x0[1], x0[2], x0[3], x1[0], x1[1], x1[2], x1[3], x2[0], x2[1], x2[2], x2[3], x3[0], x3[1], x3[2], x3[3]};
        float lo[16], hi[16], s2[16], b7[16], b9[16];
#pragma unroll
        for (int j = 2; j < 14; ++j) { lo[j] = bf_lo(xw[j]); hi[j] = bf_hi(xw[j]); s2[j] = add1(lo[j], hi[j]); }
#pragma unroll
        for (int c = 4; c < 12; ++c) {
          const float t3 = add1(add1(s2[c - 1], s2[c]), s2[c + 1]);
          b7[2 * c - 8] = add1(t3, hi[c - 2]); b7[2 * c - 7] = add1(t3, lo[c + 2]);
          b9[2 * c - 8] = add1(b7[2 * c - 7], s2[c - 2]); b9[2 * c - 7] = add1(b7[2 * c - 8], s2[c + 2]);
        }
        lptr bw = bx + 48 + 2 * l16;
#pragma unroll
        for (int j = 0; j < 2; ++j) {
          *(LAS u32x4*)(bw + 16 * j) = (u32x4){pk_bf16(b7[8 * j], b7[8 * j + 1]), pk_bf16(b7[8 * j + 2], b7[8 * j + 3]),
                                               pk_bf16(b7[8 * j + 4], b7[8 * j + 5]), pk_bf16(b7[8 * j + 6], b7[8 * j + 7])};
          *(LAS u32x4*)(bw + 2176 + 16 * j) = (u32x4){pk_bf16(b9[8 * j], b9[8 * j + 1]), pk_bf16(b9[8 * j + 2], b9[8 * j + 3]),
                                                      pk_bf16(b9[8 * j + 4], b9[8 * j + 5]), pk_bf16(b9[8 * j + 6], b9[8 * j + 7])};
        }
        if (ln < 6) *(LAS u32x4*)(bx + (ln >= 3 ? 2176 - 48 : 0) + l16) = (u32x4){0u, 0u, 0u, 0u};
        const int hh = ln >> 5, q = (ln & 15) >> 2;
        const unsigned za = L_Z + (w - 4) * 512 + 256 + 8 * ((ln & 31) - 1 - 2 * hh) - 224;
        const unsigned ba = L_BOX + (w - 4) * BOX_SZ + (q & 1) * 2176 + 128 * hh + 48 - 16 * q + 32 * ((ln >> 4) & 1) + 8 * (ln & 3);
        const unsigned yw = L_YS + (w - 4) * YS_SZ + hh * 576 + (ln & 31) * 4;
        asm volatile(
      "ds_read_b64 v[64:65], %[za] offset:224\n"
      "ds_read_b64 v[66:67], %[za] offset:232\n"
      "ds_read_b64_tr_b16 v[68:69], %[ba] offset:64\n"
      "ds_read_b64_tr_b16 v[70:71], %[ba] offset:0\n"
      "ds_read_b64 v[72:73], %[za] offset:192\n"
      "ds_read_b64 v[74:75], %[za] offset:200\n"
      "ds_read_b64_tr_b16 v[76:77], %[ba] offset:320\n"
      "ds_read_b64_tr_b16 v[78:79], %[ba] offset:256\n"
      "ds_read_b64 v[80:81], %[za] offset:160\n"
      "ds_read_b64 v[82:83], %[za] offset:168\n"
      "ds_read_b64_tr_b16 v[84:85], %[ba] offset:576\n"
      "ds_read_b64_tr_b16 v[86:87], %[ba] offset:512\n"
      "ds_read_b64 v[88:89], %[za] offset:128\n"
      "ds_read_b64 v[90:91], %[za] offset:136\n"
      "ds_read_b64_tr_b16 v[92:93], %[ba] offset:832\n"
      "ds_read_b64_tr_b16 v[94:95], %[ba] offset:768\n"
      "s_waitcnt lgkmcnt(12)\n"
      "v_mfma_f32_32x32x16_bf16 v[128:143], v[64:67], v[68:71], 0\n"
      "ds_read_b64 v[96:97], %[za] offset:96\n"
      "ds_read_b64 v[98:99], %[za] offset:104\n"
      "ds_read_b64_tr_b16 v[100:101], %[ba] offset:1088\n"
      "ds_read_b64_tr_b16 v[102:103], %[ba] offset:1024\n"
      "s_waitcnt lgkmcnt(12)\n"
      "v_mfma_f32_32x32x16_bf16 v[128:143], v[72:75], v[76:79], v[128:143]\n"
      "ds_read_b64 v[104:105], %[za] offset:64\n"
      "ds_read_b64 v[106:107], %[za] offset:72\n"
      "ds_read_b64_tr_b16 v[108:109], %[ba] offset:1344\n"
      "ds_read_b64_tr_b16 v[110:111], %[ba] offset:1280\n"
      "s_waitcnt lgkmcnt(12)\n"
      "v_mfma_f32_32x32x16_bf16 v[128:143], v[80:83], v[84:87], v[128:143]\n"
      "ds_read_b64 v[112:113], %[za] offset:32\n"
      "ds_read_b64 v[114:115], %[za] offset:40\n"
      "ds_read_b64_tr_b16 v[116:117], %[ba] offset:1600\n"
      "ds_read_b64_tr_b16 v[118:119], %[ba] offset:1536\n"
      "s_waitcnt lgkmcnt(12)\n"
      "v_mfma_f32_32x32x16_bf16 v[128:143], v[88:91], v[92:95], v[128:143]\n"
      "ds_read_b64 v[120:121], %[za] offset:0\n"
      "ds_read_b64 v[122:123], %[za] offset:8\n"
      "ds_read_b64_tr_b16 v[124:125], %[ba] offset:1856\n"
      "ds_read_b64_tr_b16 v[126:127], %[ba] offset:1792\n"
      "s_waitcnt lgkmcnt(12)\n"
      "v_mfma_f32_32x32x16_bf16 v[128:143], v[96:99], v[100:103], v[128:143]\n"
      "s_waitcnt lgkmcnt(8)\n"
      "v_mfma_f32_32x32x16_bf16 v[128:143], v[104:107], v[108:111], v[128:143]\n"
      "s_waitcnt lgkmcnt(4)\n"
      "v_mfma_f32_32x32x16_bf16 v[128:143], v[112:115], v[116:119], v[128:143]\n"
      "s_waitcnt lgkmcnt(0)\n"
      "v_mfma_f32_32x32x16_bf16 v[128:143], v[120:123], v[124:127], v[128:143]\n"
      "s_nop 15\n"
      "ds_write_b32 %[yw], v128 offset:0\n"
      "ds_write_b32 %[yw], v129 offset:144\n"
      "ds_write_b32 %[yw], v130 offset:288\n"
      "ds_write_b32 %[yw], v131 offset:432\n"
      "ds_write_b32 %[yw], v132 offset:1152\n"
      "ds_write_b32 %[yw], v133 offset:1296\n"
      "ds_write_b32 %[yw], v134 offset:1440\n"
      "ds_write_b32 %[yw], v135 offset:1584\n"
      "ds_write_b32 %[yw], v136 offset:2304\n"
      "ds_write_b32 %[yw], v137 offset:2448\n"
      "ds_write_b32 %[yw], v138 offset:2592\n"
      "ds_write_b32 %[yw], v139 offset:2736\n"
      "ds_write_b32 %[yw], v140 offset:3456\n"
      "ds_write_b32 %[yw], v141 offset:3600\n"
      "ds_write_b32 %[yw], v142 offset:3744\n"
      "ds_write_b32 %[yw], v143 offset:3888\n"
          :
          : [za] "v"(za), [ba] "v"(ba), [yw] "v"(yw)
          : "memory", "v64", "v65", "v66", "v67", "v68", "v69", V10(7), V10(8), V10(9), V10(10), V10(11), V10(12), V10(13), "v140",
            "v141", "v142", "v143");
      }
    }
    asm volatile(
      "ds_read_b64 v[100:101], %[ka] offset:576\n"
      "ds_read_b64 v[102:103], %[ka] offset:584\n"
      "ds_read_b128 v[104:107], %[ua] offset:720\n"
      "ds_read_b64 v[108:109], %[ka] offset:608\n"
      "ds_read_b64 v[110:111], %[ka] offset:616\n"
      "ds_read_b128 v[112:115], %[ua] offset:752\n"
      "ds_read_b64 v[116:117], %[ka] offset:512\n"
      "ds_read_b64 v[118:119], %[ka] offset:520\n"
      "ds_read_b128 v[120:123], %[ua] offset:640\n"
      "ds_read_b64 v[124:125], %[ka] offset:544\n"
      "ds_read_b64 v[126:127], %[ka] offset:552\n"
      "ds_read_b128 v[128:131], %[ua] offset:672\n"
      "s_waitcnt lgkmcnt(9)\n"
      "v_mfma_f32_32x32x16_bf16 %[y], v[100:103], v[104:107], 0\n"
      "ds_read_b64 v[100:101], %[ka] offset:448\n"
      "ds_read_b64 v[102:103], %[ka] offset:456\n"
      "ds_read_b128 v[104:107], %[ua] offset:560\n"
      "s_waitcnt lgkmcnt(9)\n"
      "v_mfma_f32_32x32x16_bf16 %[y], v[108:111], v[112:115], %[y]\n"
      "ds_read_b64 v[108:109], %[ka] offset:480\n"
      "ds_read_b64 v[110:111], %[ka] offset:488\n"
      "ds_read_b128 v[112:115], %[ua] offset:592\n"
      "s_waitcnt lgkmcnt(9)\n"
      "v_mfma_f32_32x32x16_bf16 %[y], v[116:119], v[120:123], %[y]\n"
      "ds_read_b64 v[116:117], %[ka] offset:384\n"
      "ds_read_b64 v[118:119], %[ka] offset:392\n"
      "ds_read_b128 v[120:123], %[ua] offset:480\n"
      "s_waitcnt lgkmcnt(9)\n"
      "v_mfma_f32_32x32x16_bf16 %[y], v[124:127], v[128:131], %[y]\n"
      "ds_read_b64 v[124:125], %[ka] offset:416\n"
      "ds_read_b64 v[126:127], %[ka] offset:424\n"
      "ds_read_b128 v[128:131], %[ua] offset:512\n"
      "s_waitcnt lgkmcnt(9)\n"
      "v_mfma_f32_32x32x16_bf16 %[y], v[100:103], v[104:107], %[y]\n"
      "ds_read_b64 v[100:101], %[ka] offset:320\n"
      "ds_read_b64 v[102:103], %[ka] offset:328\n"
      "ds_read_b128 v[104:107], %[ua] offset:400\n"
      "s_waitcnt lgkmcnt(9)\n"
      "v_mfma_f32_32x32x16_bf16 %[y], v[108:111], v[112:115], %[y]\n"
      "ds_read_b64 v[108:109], %[ka] offset:352\n"
      "ds_read_b64 v[110:111], %[ka] offset:360\n"
      "ds_read_b128 v[112:115], %[ua] offset:432\n"
      "s_waitcnt lgkmcnt(9)\n"
      "v_mfma_f32_32x32x16_bf16 %[y], v[116:119], v[120:123], %[y]\n"
      "ds_read_b64 v[116:117], %[ka] offset:256\n"
      "ds_read_b64 v[118:119], %[ka] offset:264\n"
      "ds_read_b128 v[120:123], %[ua] offset:320\n"
      "s_waitcnt lgkmcnt(9)\n"
      "v_mfma_f32_32x32x16_bf16 %[y], v[124:127], v[128:131], %[y]\n"
      "ds_read_b64 v[124:125], %[ka] offset:288\n"
      "ds_read_b64 v[126:127], %[ka] offset:296\n"
      "ds_read_b128 v[128:131], %[ua] offset:352\n"
      "s_waitcnt lgkmcnt(9)\n"
      "v_mfma_f32_32x32x16_bf16 %[y], v[100:103], v[104:107], %[y]\n"
      "ds_read_b64 v[100:101], %[ka] offset:192\n"
      "ds_read_b64 v[102:103], %[ka] offset:200\n"
      "ds_read_b128 v[104:107], %[ua] offset:240\n"
      "s_waitcnt lgkmcnt(9)\n"
      "v_mfma_f32_32x32x16_bf16 %[y], v[108:111], v[112:115], %[y]\n"
      "ds_read_b64 v[108:109], %[ka] offset:224\n"
      "ds_read_b64 v[110:111], %[ka] offset:232\n"
      "ds_read_b128 v[112:115], %[ua] offset:272\n"
      "s_waitcnt lgkmcnt(9)\n"
      "v_mfma_f32_32x32x16_bf16 %[y], v[116:119], v[120:123], %[y]\n"
      "ds_read_b64 v[116:117], %[ka] offset:128\n"
      "ds_read_b64 v[118:119], %[ka] offset:136\n"
      "ds_read_b128 v[120:123], %[ua] offset:160\n"
      "s_waitcnt lgkmcnt(9)\n"
      "v_mfma_f32_32x32x16_bf16 %[y], v[124:127], v[128:131], %[y]\n"
      "ds_read_b64 v[124:125], %[ka] offset:160\n"
      "ds_read_b64 v[126:127], %[ka] offset:168\n"
      "ds_read_b128 v[128:131], %[ua] offset:192\n"
      "s_waitcnt lgkmcnt(9)\n"
      "v_mfma_f32_32x32x16_bf16 %[y], v[100:103], v[104:107], %[y]\n"
      "ds_read_b64 v[100:101], %[ka] offset:64\n"
      "ds_read_b64 v[102:103], %[ka] offset:72\n"
      "ds_read_b128 v[104:107], %[ua] offset:80\n"
      "s_waitcnt lgkmcnt(9)\n"
      "v_mfma_f32_32x32x16_bf16 %[y], v[108:111], v[112:115], %[y]\n"
      "ds_read_b64 v[108:109], %[ka] offset:96\n"
      "ds_read_b64 v[110:111], %[ka] offset:104\n"
      "ds_read_b128 v[112:115], %[ua] offset:112\n"
      "s_waitcnt lgkmcnt(9)\n"
      "v_mfma_f32_32x32x16_bf16 %[y], v[116:119], v[120:123], %[y]\n"
      "ds_read_b64 v[116:117], %[ka] offset:0\n"
      "ds_read_b64 v[118:119], %[ka] offset:8\n"
      "ds_read_b128 v[120:123], %[ua] offset:0\n"
      "s_waitcnt lgkmcnt(9)\n"
      "v_mfma_f32_32x32x16_bf16 %[y], v[124:127], v[128:131], %[y]\n"
      "ds_read_b64 v[124:125], %[ka] offset:32\n"
      "ds_read_b64 v[126:127], %[ka] offset:40\n"
      "ds_read_b128 v[128:131], %[ua] offset:32\n"
      "s_cmp_lt_u32 %[w], 7\n"
      "s_cbranch_scc1 1f\n"
      "s_waitcnt lgkmcnt(9)\n"
      "v_mfma_f32_32x32x16_bf16 %[y], v[100:103], v[104:107], %[y]\n"
      "s_waitcnt lgkmcnt(6)\n"
      "v_mfma_f32_32x32x16_bf16 %[y], v[108:111], v[112:115], %[y]\n"
      "s_waitcnt lgkmcnt(3)\n"
      "v_mfma_f32_32x32x16_bf16 %[y], v[116:119], v[120:123], %[y]\n"
      "s_waitcnt lgkmcnt(0)\n"
      "v_mfma_f32_32x32x16_bf16 %[y], v[124:127], v[128:131], %[y]\n"
      "1:\n"
      "s_waitcnt lgkmcnt(0)\n"
      "s_nop 15\n"
      : [y] "=&{v[154:169]}"(wr0)
      : [ka] "{v20}"(ka), [ua] "{v21}"(ua), [w] "s"(w)
      : "memory", "scc", V10(10), V10(11), V10(12), "v130", "v131");
    if (w < 7) {
#pragma unroll
      for (int i = 0; i < 4; ++i) *(LAS f32x4*)(bx + l16 + i * 1024) = (f32x4){wr0[4 * i], wr0[4 * i + 1], wr0[4 * i + 2], wr0[4 * i + 3]};
    }
  }

#define WR(m0, m) m0 "{v[154:169]}"(wr0), m "{v[170:185]}"(wr1), m "{v[186:201]}"(wr2), m "{v[202:217]}"(wr3), m "{v[218:233]}"(wr4), \
              m "{v[234:249]}"(wr5), m "{v[250:253]}"(wr6)
  {
    const unsigned m0c = w * KV_PAIR, m0d = (w & 3) * 1024, ix = 0xdddd;
    asm volatile(
      "v_lshrrev_b32 v60, 1, %[l16]\n"
      "v_add_u32 v60, 0x800, v60\n"
      "v_mov_b32 v61, %[av]\n"
      "v_add_u32 v62, 67584, %[l16]\n"
      "v_add_u32 v63, 67584, v60\n"
      "v_add_u32 %[av], 67584, %[av]\n"
      "v_add_u32 v59, 0x10000, %[l16]\n"
      "v_add_u32 v56, %[qm], %[l16]\n"
      "v_add_u32 v57, %[qn], %[r8]\n"
      "v_add_u32 %[g], 0x8000, %[g]\n"
      "v_mov_b64 v[40:41], 0\n"
      "v_mov_b64 v[42:43], 0\n"
      "v_mov_b64 v[44:45], 0\n"
      "v_mov_b64 v[46:47], 0\n"
      "v_mov_b64 v[48:49], 0\n"
      "v_mov_b64 v[50:51], 0\n"
      "v_mov_b64 v[52:53], 0\n"
      "v_mov_b64 v[54:55], 0\n"
      "s_waitcnt vmcnt(8) lgkmcnt(0)\n"
      "s_barrier\n"
      "ds_read_b128 v[34:37], v56\n"
      "ds_read_b64 v[38:39], v57\n"
      "s_setprio 1\n"
      "ds_read_b64 v[152:153], v60\n"
      "ds_read_b128 v[144:147], %[l16]\n"
      "ds_read_b128 v[148:151], %[l16] offset:1024\n"
      "s_bitcmp1_b32 %[grp], 2\n"
      "s_cbranch_scc0 9f\n"
      "ds_read_b128 v[170:173], v59 offset:32768\n"
      "ds_read_b128 v[174:177], v59 offset:33792\n"
      "ds_read_b128 v[178:181], v59 offset:34816\n"
      "ds_read_b128 v[182:185], v59 offset:35840\n"
      "ds_read_b128 v[186:189], v59 offset:37120\n"
      "ds_read_b128 v[190:193], v59 offset:38144\n"
      "ds_read_b128 v[194:197], v59 offset:39168\n"
      "ds_read_b128 v[198:201], v59 offset:40192\n"
      "ds_read_b128 v[202:205], v59 offset:41472\n"
      "ds_read_b128 v[206:209], v59 offset:42496\n"
      "ds_read_b128 v[210:213], v59 offset:43520\n"
      "ds_read_b128 v[214:217], v59 offset:44544\n"
      "s_waitcnt lgkmcnt(8)\n"
      "v_pk_add_f32 v[154:155], v[154:155], v[170:171]\n"
      "v_pk_add_f32 v[156:157], v[156:157], v[172:173]\n"
      "v_pk_add_f32 v[158:159], v[158:159], v[174:175]\n"
      "v_pk_add_f32 v[160:161], v[160:161], v[176:177]\n"
      "v_pk_add_f32 v[162:163], v[162:163], v[178:179]\n"
      "v_pk_add_f32 v[164:165], v[164:165], v[180:181]\n"
      "v_pk_add_f32 v[166:167], v[166:167], v[182:183]\n"
      "v_pk_add_f32 v[168:169], v[168:169], v[184:185]\n"
      "s_waitcnt lgkmcnt(4)\n"
      "v_pk_add_f32 v[154:155], v[154:155], v[186:187]\n"
      "v_pk_add_f32 v[156:157], v[156:157], v[188:189]\n"
      "v_pk_add_f32 v[158:159], v[158:159], v[190:191]\n"
      "v_pk_add_f32 v[160:161], v[160:161], v[192:193]\n"
      "v_pk_add_f32 v[162:163], v[162:163], v[194:195]\n"
      "v_pk_add_f32 v[164:165], v[164:165], v[196:197]\n"
      "v_pk_add_f32 v[166:167], v[166:167], v[198:199]\n"
      "v_pk_add_f32 v[168:169], v[168:169], v[200:201]\n"
      "s_waitcnt lgkmcnt(0)\n"
      "v_pk_add_f32 v[154:155], v[154:155], v[202:203]\n"
      "v_pk_add_f32 v[156:157], v[156:157], v[204:205]\n"
      "v_pk_add_f32 v[158:159], v[158:159], v[206:207]\n"
      "v_pk_add_f32 v[160:161], v[160:161], v[208:209]\n"
      "v_pk_add_f32 v[162:163], v[162:163], v[210:211]\n"
      "v_pk_add_f32 v[164:165], v[164:165], v[212:213]\n"
      "v_pk_add_f32 v[166:167], v[166:167], v[214:215]\n"
      "v_pk_add_f32 v[168:169], v[168:169], v[216:217]\n"
      "9:\n"
      "s_bitcmp1_b32 %[grp], 1\n"
      "s_cbranch_scc0 7f\n"
      "ds_read_b128 v[96:99], %[ys] offset:0\n"
      "ds_read_b128 v[100:103], %[ys] offset:32\n"
      "ds_read_b128 v[104:107], %[ys] offset:64\n"
      "ds_read_b128 v[108:111], %[ys] offset:96\n"
      "s_waitcnt lgkmcnt(0)\n"
      "v_pk_add_f32 v[154:155], v[154:155], v[96:97]\n"
      "v_pk_add_f32 v[156:157], v[156:157], v[98:99]\n"
      "v_pk_add_f32 v[158:159], v[158:159], v[100:101]\n"
      "v_pk_add_f32 v[160:161], v[160:161], v[102:103]\n"
      "v_pk_add_f32 v[162:163], v[162:163], v[104:105]\n"
      "v_pk_add_f32 v[164:165], v[164:165], v[106:107]\n"
      "v_pk_add_f32 v[166:167], v[166:167], v[108:109]\n"
      "v_pk_add_f32 v[168:169], v[168:169], v[110:111]\n"
      "7:\n"
      "s_waitcnt lgkmcnt(0)\n"
      "v_mfma_f32_32x32x4_2b_bf16 v[64:95], v[152:153], v[38:39], 0\n"
      "ds_read_b64 v[152:153], v60 offset:5632\n"
      "ds_read_b128 v[128:131], v61\n"
      "ds_read_b128 v[132:135], v61 offset:768\n"
      "v_cvt_pk_bf16_f32 v96, v154, v155\n"
      "v_cvt_pk_bf16_f32 v97, v156, v157\n"
      "v_cvt_pk_bf16_f32 v98, v158, v159\n"
      "v_cvt_pk_bf16_f32 v99, v160, v161\n"
      "v_cvt_pk_bf16_f32 v100, v162, v163\n"
      "v_cvt_pk_bf16_f32 v101, v164, v165\n"
      "v_cvt_pk_bf16_f32 v102, v166, v167\n"
      "v_cvt_pk_bf16_f32 v103, v168, v169\n"
      "ds_write_b64 %[tw], v[96:97] offset:0\n"
      "ds_write_b64 %[tw], v[98:99] offset:16\n"
      "ds_write_b64 %[tw], v[100:101] offset:32\n"
      "ds_write_b64 %[tw], v[102:103] offset:48\n"
      "ds_read_b128 v[104:107], %[tr]\n"
      "ds_read_b128 v[108:111], %[tr] offset:1280\n"
      "s_nop 0\n"
      "v_mfma_f32_32x32x16_bf16 v[64:79], v[144:147], v[34:37], v[64:79]\n"
      "ds_read_b128 v[144:147], %[l16] offset:5632\n"
      "v_mfma_f32_32x32x16_bf16 v[80:95], v[148:151], v[34:37], v[80:95]\n"
      "ds_read_b128 v[148:151], %[l16] offset:6656\n"
      "ds_read_b128 v[136:139], v61 offset:1536\n"
      "s_waitcnt lgkmcnt(3)\n"
      "s_bitcmp1_b32 %[grp], 0\n"
      "s_cbranch_scc1 8f\n"
      "global_store_dwordx4 %[yo], v[104:107], %[ws] sc1\n"
      "global_store_dwordx4 %[yo], v[108:111], %[ws] offset:1024 sc1\n"
      "8:\n"
      "s_nop 4\n"
      "v_exp_f32 v64, v64\n"
      "v_exp_f32 v65, v65\n"
      "s_waitcnt lgkmcnt(0)\n"
      "ds_read_b128 v[140:143], v61 offset:2304\n"
      "v_exp_f32 v66, v66\n"
      "v_exp_f32 v67, v67\n"
      "v_exp_f32 v68, v68\n"
      "v_mfma_f32_32x32x4_2b_bf16 v[96:127], v[152:153], v[38:39], 0\n"
      "ds_read_b64 v[152:153], v60 offset:11264\n"
      "v_exp_f32 v69, v69\n"
      "v_exp_f32 v70, v70\n"
      "v_exp_f32 v71, v71\n"
      "v_exp_f32 v72, v72\n"
      "v_exp_f32 v73, v73\n"
      "v_exp_f32 v74, v74\n"
      "v_exp_f32 v75, v75\n"
      "v_exp_f32 v76, v76\n"
      "v_smfmac_f32_32x32x32_bf16 v[40:55], v[128:131], v[64:71], %[ix]\n"
      "ds_read_b128 v[128:131], v61 offset:5632\n"
      "v_exp_f32 v77, v77\n"
      "v_exp_f32 v78, v78\n"
      "v_exp_f32 v79, v79\n"
      "v_exp_f32 v80, v80\n"
      "v_exp_f32 v81, v81\n"
      "v_smfmac_f32_32x32x32_bf16 v[40:55], v[132:135], v[72:79], %[ix]\n"
      "ds_read_b128 v[132:135], v61 offset:6400\n"
      "v_exp_f32 v82, v82\n"
      "v_exp_f32 v83, v83\n"
      "v_exp_f32 v84, v84\n"
      "v_mfma_f32_32x32x16_bf16 v[96:111], v[144:147], v[34:37], v[96:111]\n"
      "ds_read_b128 v[144:147], %[l16] offset:11264\n"
      "v_exp_f32 v85, v85\n"
      "v_exp_f32 v86, v86\n"
      "v_exp_f32 v87, v87\n"
      "v_exp_f32 v88, v88\n"
      "v_mfma_f32_32x32x16_bf16 v[112:127], v[148:151], v[34:37], v[112:127]\n"
      "ds_read_b128 v[148:151], %[l16] offset:12288\n"
      "v_exp_f32 v89, v89\n"
      "v_exp_f32 v90, v90\n"
      "v_exp_f32 v91, v91\n"
      "v_smfmac_f32_32x32x32_bf16 v[40:55], v[136:139], v[80:87], %[ix]\n"
      "ds_read_b128 v[136:139], v61 offset:7168\n"
      "s_add_u32 s44, %[m0b], 65536\n"
      "s_mov_b32 m0, s44\n"
      "v_add_u32 %[g], 0x8000, %[g]\n"
      "global_load_lds_dwordx4 %[g], %[kv] offset:0 sc1\n"
      "v_exp_f32 v92, v92\n"
      "v_exp_f32 v93, v93\n"
      "v_exp_f32 v94, v94\n"
      "v_exp_f32 v95, v95\n"
      "v_exp_f32 v96, v96\n"
      "v_exp_f32 v97, v97\n"
      "s_waitcnt lgkmcnt(0)\n"
      "v_smfmac_f32_32x32x32_bf16 v[40:55], v[140:143], v[88:95], %[ix]\n"
      "ds_read_b128 v[140:143], v61 offset:7936\n"
      "v_exp_f32 v98, v98\n"
      "v_exp_f32 v99, v99\n"
      "v_exp_f32 v100, v100\n"
      "v_mfma_f32_32x32x4_2b_bf16 v[64:95], v[152:153], v[38:39], 0\n"
      "ds_read_b64 v[152:153], v60 offset:16896\n"
      "v_exp_f32 v101, v101\n"
      "v_exp_f32 v102, v102\n"
      "v_exp_f32 v103, v103\n"
      "v_exp_f32 v104, v104\n"
      "v_exp_f32 v105, v105\n"
      "v_exp_f32 v106, v106\n"
      "v_exp_f32 v107, v107\n"
      "v_exp_f32 v108, v108\n"
      "v_smfmac_f32_32x32x32_bf16 v[40:55], v[128:131], v[96:103], %[ix]\n"
      "ds_read_b128 v[128:131], v61 offset:11264\n"
      "v_exp_f32 v109, v109\n"
      "v_exp_f32 v110, v110\n"
      "v_exp_f32 v111, v111\n"
      "v_exp_f32 v112, v112\n"
      "v_exp_f32 v113, v113\n"
      "v_smfmac_f32_32x32x32_bf16 v[40:55], v[132:135], v[104:111], %[ix]\n"
      "ds_read_b128 v[132:135], v61 offset:12032\n"
      "v_exp_f32 v114, v114\n"
      "v_exp_f32 v115, v115\n"
      "v_exp_f32 v116, v116\n"
      "v_mfma_f32_32x32x16_bf16 v[64:79], v[144:147], v[34:37], v[64:79]\n"
      "ds_read_b128 v[144:147], %[l16] offset:16896\n"
      "v_exp_f32 v117, v117\n"
      "v_exp_f32 v118, v118\n"
      "v_exp_f32 v119, v119\n"
      "v_exp_f32 v120, v120\n"
      "v_mfma_f32_32x32x16_bf16 v[80:95], v[148:151], v[34:37], v[80:95]\n"
      "ds_read_b128 v[148:151], %[l16] offset:17920\n"
      "v_exp_f32 v121, v121\n"
      "v_exp_f32 v122, v122\n"
      "v_exp_f32 v123, v123\n"
      "v_smfmac_f32_32x32x32_bf16 v[40:55], v[136:139], v[112:119], %[ix]\n"
      "ds_read_b128 v[136:139], v61 offset:12800\n"
      "global_load_lds_dwordx4 %[g], %[kv] offset:1024 sc1\n"
      "v_exp_f32 v124, v124\n"
      "v_exp_f32 v125, v125\n"
      "v_exp_f32 v126, v126\n"
      "v_exp_f32 v127, v127\n"
      "v_exp_f32 v64, v64\n"
      "v_exp_f32 v65, v65\n"
      "s_waitcnt lgkmcnt(0)\n"
      "v_smfmac_f32_32x32x32_bf16 v[40:55], v[140:143], v[120:127], %[ix]\n"
      "ds_read_b128 v[140:143], v61 offset:13568\n"
      "v_exp_f32 v66, v66\n"
      "v_exp_f32 v67, v67\n"
      "v_exp_f32 v68, v68\n"
      "v_mfma_f32_32x32x4_2b_bf16 v[96:127], v[152:153], v[38:39], 0\n"
      "ds_read_b64 v[152:153], v60 offset:22528\n"
      "v_exp_f32 v69, v69\n"
      "v_exp_f32 v70, v70\n"
      "v_exp_f32 v71, v71\n"
      "v_exp_f32 v72, v72\n"
      "v_exp_f32 v73, v73\n"
      "v_exp_f32 v74, v74\n"
      "v_exp_f32 v75, v75\n"
      "v_exp_f32 v76, v76\n"
      "v_smfmac_f32_32x32x32_bf16 v[40:55], v[128:131], v[64:71], %[ix]\n"
      "ds_read_b128 v[128:131], v61 offset:16896\n"
      "v_exp_f32 v77, v77\n"
      "v_exp_f32 v78, v78\n"
      "v_exp_f32 v79, v79\n"
      "v_exp_f32 v80, v80\n"
      "v_exp_f32 v81, v81\n"
      "v_smfmac_f32_32x32x32_bf16 v[40:55], v[132:135], v[72:79], %[ix]\n"
      "ds_read_b128 v[132:135], v61 offset:17664\n"
      "v_exp_f32 v82, v82\n"
      "v_exp_f32 v83, v83\n"
      "v_exp_f32 v84, v84\n"
      "v_mfma_f32_32x32x16_bf16 v[96:111], v[144:147], v[34:37], v[96:111]\n"
      "ds_read_b128 v[144:147], %[l16] offset:22528\n"
      "v_exp_f32 v85, v85\n"
      "v_exp_f32 v86, v86\n"
      "v_exp_f32 v87, v87\n"
      "v_exp_f32 v88, v88\n"
      "v_mfma_f32_32x32x16_bf16 v[112:127], v[148:151], v[34:37], v[112:127]\n"
      "ds_read_b128 v[148:151], %[l16] offset:23552\n"
      "v_exp_f32 v89, v89\n"
      "v_exp_f32 v90, v90\n"
      "v_exp_f32 v91, v91\n"
      "v_smfmac_f32_32x32x32_bf16 v[40:55], v[136:139], v[80:87], %[ix]\n"
      "ds_read_b128 v[136:139], v61 offset:18432\n"
      "global_load_lds_dwordx4 %[g], %[kv] offset:2048 sc1\n"
      "v_exp_f32 v92, v92\n"
      "v_exp_f32 v93, v93\n"
      "v_exp_f32 v94, v94\n"
      "v_exp_f32 v95, v95\n"
      "v_exp_f32 v96, v96\n"
      "v_exp_f32 v97, v97\n"
      "s_waitcnt lgkmcnt(0)\n"
      "v_smfmac_f32_32x32x32_bf16 v[40:55], v[140:143], v[88:95], %[ix]\n"
      "ds_read_b128 v[140:143], v61 offset:19200\n"
      "v_exp_f32 v98, v98\n"
      "v_exp_f32 v99, v99\n"
      "v_exp_f32 v100, v100\n"
      "v_mfma_f32_32x32x4_2b_bf16 v[64:95], v[152:153], v[38:39], 0\n"
      "ds_read_b64 v[152:153], v60 offset:28160\n"
      "v_exp_f32 v101, v101\n"
      "v_exp_f32 v102, v102\n"
      "v_exp_f32 v103, v103\n"
      "v_exp_f32 v104, v104\n"
      "v_exp_f32 v105, v105\n"
      "v_exp_f32 v106, v106\n"
      "v_exp_f32 v107, v107\n"
      "v_exp_f32 v108, v108\n"
      "v_smfmac_f32_32x32x32_bf16 v[40:55], v[128:131], v[96:103], %[ix]\n"
      "ds_read_b128 v[128:131], v61 offset:22528\n"
      "v_exp_f32 v109, v109\n"
      "v_exp_f32 v110, v110\n"
      "v_exp_f32 v111, v111\n"
      "v_exp_f32 v112, v112\n"
      "v_exp_f32 v113, v113\n"
      "v_smfmac_f32_32x32x32_bf16 v[40:55], v[132:135], v[104:111], %[ix]\n"
      "ds_read_b128 v[132:135], v61 offset:23296\n"
      "v_exp_f32 v114, v114\n"
      "v_exp_f32 v115, v115\n"
      "v_exp_f32 v116, v116\n"
      "v_mfma_f32_32x32x16_bf16 v[64:79], v[144:147], v[34:37], v[64:79]\n"
      "ds_read_b128 v[144:147], %[l16] offset:28160\n"
      "v_exp_f32 v117, v117\n"
      "v_exp_f32 v118, v118\n"
      "v_exp_f32 v119, v119\n"
      "v_exp_f32 v120, v120\n"
      "v_mfma_f32_32x32x16_bf16 v[80:95], v[148:151], v[34:37], v[80:95]\n"
      "ds_read_b128 v[148:151], %[l16] offset:29184\n"
      "v_exp_f32 v121, v121\n"
      "v_exp_f32 v122, v122\n"
      "v_exp_f32 v123, v123\n"
      "v_smfmac_f32_32x32x32_bf16 v[40:55], v[136:139], v[112:119], %[ix]\n"
      "ds_read_b128 v[136:139], v61 offset:24064\n"
      "global_load_lds_dwordx4 %[g], %[kv] offset:3072 sc1\n"
      "v_exp_f32 v124, v124\n"
      "v_exp_f32 v125, v125\n"
      "v_exp_f32 v126, v126\n"
      "v_exp_f32 v127, v127\n"
      "s_waitcnt vmcnt(4)\n"
      "s_barrier\n"
      "s_cmp_eq_u32 %[w], 0\n"
      "s_cbranch_scc0 2f\n"
      "s_mov_b64 exec, 1\n"
      "v_mov_b32 v59, %[f3]\n"
      "global_store_dwordx2 v59, %[tag], %[ws] sc1\n"
      "s_mov_b64 exec, -1\n"
      "2:\n"
      "v_exp_f32 v64, v64\n"
      "v_exp_f32 v65, v65\n"
      "s_waitcnt lgkmcnt(0)\n"
      "v_smfmac_f32_32x32x32_bf16 v[40:55], v[140:143], v[120:127], %[ix]\n"
      "ds_read_b128 v[140:143], v61 offset:24832\n"
      "v_exp_f32 v66, v66\n"
      "v_exp_f32 v67, v67\n"
      "v_exp_f32 v68, v68\n"
      "v_mfma_f32_32x32x4_2b_bf16 v[96:127], v[152:153], v[38:39], 0\n"
      "ds_read_b64 v[152:153], v60 offset:33792\n"
      "v_exp_f32 v69, v69\n"
      "v_exp_f32 v70, v70\n"
      "v_exp_f32 v71, v71\n"
      "v_exp_f32 v72, v72\n"
      "v_exp_f32 v73, v73\n"
      "v_exp_f32 v74, v74\n"
      "v_exp_f32 v75, v75\n"
      "v_exp_f32 v76, v76\n"
      "v_smfmac_f32_32x32x32_bf16 v[40:55], v[128:131], v[64:71], %[ix]\n"
      "ds_read_b128 v[128:131], v61 offset:28160\n"
      "s_add_u32 s44, %[m0d], 0x20000\n"
      "s_mov_b32 m0, s44\n"
      "v_add_u32 v59, s44, %[l16]\n"
      "global_load_lds_dwordx4 v59, %[kv] sc1\n"
      "v_exp_f32 v77, v77\n"
      "v_exp_f32 v78, v78\n"
      "v_exp_f32 v79, v79\n"
      "v_exp_f32 v80, v80\n"
      "v_exp_f32 v81, v81\n"
      "v_smfmac_f32_32x32x32_bf16 v[40:55], v[132:135], v[72:79], %[ix]\n"
      "ds_read_b128 v[132:135], v61 offset:28928\n"
      "s_add_u32 s44, %[m0b], 98304\n"
      "s_mov_b32 m0, s44\n"
      "v_add_u32 %[g], 0x8000, %[g]\n"
      "global_load_lds_dwordx4 %[g], %[kv] offset:0 sc1\n"
      "v_exp_f32 v82, v82\n"
      "v_exp_f32 v83, v83\n"
      "v_exp_f32 v84, v84\n"
      "v_mfma_f32_32x32x16_bf16 v[96:111], v[144:147], v[34:37], v[96:111]\n"
      "ds_read_b128 v[144:147], %[l16] offset:33792\n"
      "v_exp_f32 v85, v85\n"
      "v_exp_f32 v86, v86\n"
      "v_exp_f32 v87, v87\n"
      "v_exp_f32 v88, v88\n"
      "v_mfma_f32_32x32x16_bf16 v[112:127], v[148:151], v[34:37], v[112:127]\n"
      "ds_read_b128 v[148:151], %[l16] offset:34816\n"
      "v_exp_f32 v89, v89\n"
      "v_exp_f32 v90, v90\n"
      "v_exp_f32 v91, v91\n"
      "v_smfmac_f32_32x32x32_bf16 v[40:55], v[136:139], v[80:87], %[ix]\n"
      "ds_read_b128 v[136:139], v61 offset:29696\n"
      "global_load_lds_dwordx4 %[g], %[kv] offset:1024 sc1\n"
      "v_exp_f32 v92, v92\n"
      "v_exp_f32 v93, v93\n"
      "v_exp_f32 v94, v94\n"
      "v_exp_f32 v95, v95\n"
      "v_exp_f32 v96, v96\n"
      "v_exp_f32 v97, v97\n"
      "s_waitcnt lgkmcnt(0)\n"
      "v_smfmac_f32_32x32x32_bf16 v[40:55], v[140:143], v[88:95], %[ix]\n"
      "ds_read_b128 v[140:143], v61 offset:30464\n"
      "v_exp_f32 v98, v98\n"
      "v_exp_f32 v99, v99\n"
      "v_exp_f32 v100, v100\n"
      "v_mfma_f32_32x32x4_2b_bf16 v[64:95], v[152:153], v[38:39], 0\n"
      "ds_read_b64 v[152:153], v60 offset:39424\n"
      "v_exp_f32 v101, v101\n"
      "v_exp_f32 v102, v102\n"
      "v_exp_f32 v103, v103\n"
      "v_exp_f32 v104, v104\n"
      "v_exp_f32 v105, v105\n"
      "v_exp_f32 v106, v106\n"
      "v_exp_f32 v107, v107\n"
      "v_exp_f32 v108, v108\n"
      "v_smfmac_f32_32x32x32_bf16 v[40:55], v[128:131], v[96:103], %[ix]\n"
      "ds_read_b128 v[128:131], v61 offset:33792\n"
      "v_exp_f32 v109, v109\n"
      "v_exp_f32 v110, v110\n"
      "v_exp_f32 v111, v111\n"
      "v_exp_f32 v112, v112\n"
      "v_exp_f32 v113, v113\n"
      "v_smfmac_f32_32x32x32_bf16 v[40:55], v[132:135], v[104:111], %[ix]\n"
      "ds_read_b128 v[132:135], v61 offset:34560\n"
      "global_load_lds_dwordx4 %[g], %[kv] offset:2048 sc1\n"
      "v_exp_f32 v114, v114\n"
      "v_exp_f32 v115, v115\n"
      "v_exp_f32 v116, v116\n"
      "v_mfma_f32_32x32x16_bf16 v[64:79], v[144:147], v[34:37], v[64:79]\n"
      "ds_read_b128 v[144:147], %[l16] offset:39424\n"
      "v_exp_f32 v117, v117\n"
      "v_exp_f32 v118, v118\n"
      "v_exp_f32 v119, v119\n"
      "v_exp_f32 v120, v120\n"
      "v_mfma_f32_32x32x16_bf16 v[80:95], v[148:151], v[34:37], v[80:95]\n"
      "ds_read_b128 v[148:151], %[l16] offset:40448\n"
      "v_exp_f32 v121, v121\n"
      "v_exp_f32 v122, v122\n"
      "v_exp_f32 v123, v123\n"
      "v_smfmac_f32_32x32x32_bf16 v[40:55], v[136:139], v[112:119], %[ix]\n"
      "ds_read_b128 v[136:139], v61 offset:35328\n"
      "global_load_lds_dwordx4 %[g], %[kv] offset:3072 sc1\n"
      "v_exp_f32 v124, v124\n"
      "v_exp_f32 v125, v125\n"
      "v_exp_f32 v126, v126\n"
      "v_exp_f32 v127, v127\n"
      "v_exp_f32 v64, v64\n"
      "v_exp_f32 v65, v65\n"
      "s_waitcnt lgkmcnt(0)\n"
      "v_smfmac_f32_32x32x32_bf16 v[40:55], v[140:143], v[120:127], %[ix]\n"
      "ds_read_b128 v[140:143], v61 offset:36096\n"
      "v_exp_f32 v66, v66\n"
      "v_exp_f32 v67, v67\n"
      "v_exp_f32 v68, v68\n"
      "v_mfma_f32_32x32x4_2b_bf16 v[96:127], v[152:153], v[38:39], 0\n"
      "ds_read_b64 v[152:153], v60 offset:45056\n"
      "v_exp_f32 v69, v69\n"
      "v_exp_f32 v70, v70\n"
      "v_exp_f32 v71, v71\n"
      "v_exp_f32 v72, v72\n"
      "v_exp_f32 v73, v73\n"
      "v_exp_f32 v74, v74\n"
      "v_exp_f32 v75, v75\n"
      "v_exp_f32 v76, v76\n"
      "v_smfmac_f32_32x32x32_bf16 v[40:55], v[128:131], v[64:71], %[ix]\n"
      "ds_read_b128 v[128:131], v61 offset:39424\n"
      "v_exp_f32 v77, v77\n"
      "v_exp_f32 v78, v78\n"
      "v_exp_f32 v79, v79\n"
      "v_exp_f32 v80, v80\n"
      "v_exp_f32 v81, v81\n"
      "v_smfmac_f32_32x32x32_bf16 v[40:55], v[132:135], v[72:79], %[ix]\n"
      "ds_read_b128 v[132:135], v61 offset:40192\n"
      "v_exp_f32 v82, v82\n"
      "v_exp_f32 v83, v83\n"
      "v_exp_f32 v84, v84\n"
      "v_mfma_f32_32x32x16_bf16 v[96:111], v[144:147], v[34:37], v[96:111]\n"
      "ds_read_b128 v[144:147], %[l16] offset:45056\n"
      "v_exp_f32 v85, v85\n"
      "v_exp_f32 v86, v86\n"
      "v_exp_f32 v87, v87\n"
      "v_exp_f32 v88, v88\n"
      "v_mfma_f32_32x32x16_bf16 v[112:127], v[148:151], v[34:37], v[112:127]\n"
      "ds_read_b128 v[148:151], %[l16] offset:46080\n"
      "v_exp_f32 v89, v89\n"
      "v_exp_f32 v90, v90\n"
      "v_exp_f32 v91, v91\n"
      "v_smfmac_f32_32x32x32_bf16 v[40:55], v[136:139], v[80:87], %[ix]\n"
      "ds_read_b128 v[136:139], v61 offset:40960\n"
      "v_exp_f32 v92, v92\n"
      "v_exp_f32 v93, v93\n"
      "v_exp_f32 v94, v94\n"
      "v_exp_f32 v95, v95\n"
      "v_exp_f32 v96, v96\n"
      "v_exp_f32 v97, v97\n"
      "s_waitcnt lgkmcnt(0)\n"
      "v_smfmac_f32_32x32x32_bf16 v[40:55], v[140:143], v[88:95], %[ix]\n"
      "ds_read_b128 v[140:143], v61 offset:41728\n"
      "v_exp_f32 v98, v98\n"
      "v_exp_f32 v99, v99\n"
      "v_exp_f32 v100, v100\n"
      "v_mfma_f32_32x32x4_2b_bf16 v[64:95], v[152:153], v[38:39], 0\n"
      "ds_read_b64 v[152:153], v60 offset:50688\n"
      "v_exp_f32 v101, v101\n"
      "v_exp_f32 v102, v102\n"
      "v_exp_f32 v103, v103\n"
      "v_exp_f32 v104, v104\n"
      "v_exp_f32 v105, v105\n"
      "v_exp_f32 v106, v106\n"
      "v_exp_f32 v107, v107\n"
      "v_exp_f32 v108, v108\n"
      "v_smfmac_f32_32x32x32_bf16 v[40:55], v[128:131], v[96:103], %[ix]\n"
      "ds_read_b128 v[128:131], v61 offset:45056\n"
      "v_exp_f32 v109, v109\n"
      "v_exp_f32 v110, v110\n"
      "v_exp_f32 v111, v111\n"
      "v_exp_f32 v112, v112\n"
      "v_exp_f32 v113, v113\n"
      "v_smfmac_f32_32x32x32_bf16 v[40:55], v[132:135], v[104:111], %[ix]\n"
      "ds_read_b128 v[132:135], v61 offset:45824\n"
      "v_exp_f32 v114, v114\n"
      "v_exp_f32 v115, v115\n"
      "v_exp_f32 v116, v116\n"
      "v_mfma_f32_32x32x16_bf16 v[64:79], v[144:147], v[34:37], v[64:79]\n"
      "ds_read_b128 v[144:147], %[l16] offset:50688\n"
      "v_exp_f32 v117, v117\n"
      "v_exp_f32 v118, v118\n"
      "v_exp_f32 v119, v119\n"
      "v_exp_f32 v120, v120\n"
      "v_mfma_f32_32x32x16_bf16 v[80:95], v[148:151], v[34:37], v[80:95]\n"
      "ds_read_b128 v[148:151], %[l16] offset:51712\n"
      "v_exp_f32 v121, v121\n"
      "v_exp_f32 v122, v122\n"
      "v_exp_f32 v123, v123\n"
      "v_smfmac_f32_32x32x32_bf16 v[40:55], v[136:139], v[112:119], %[ix]\n"
      "ds_read_b128 v[136:139], v61 offset:46592\n"
      "v_exp_f32 v124, v124\n"
      "v_exp_f32 v125, v125\n"
      "v_exp_f32 v126, v126\n"
      "v_exp_f32 v127, v127\n"
      "v_exp_f32 v64, v64\n"
      "v_exp_f32 v65, v65\n"
      "s_waitcnt lgkmcnt(0)\n"
      "v_smfmac_f32_32x32x32_bf16 v[40:55], v[140:143], v[120:127], %[ix]\n"
      "ds_read_b128 v[140:143], v61 offset:47360\n"
      "v_exp_f32 v66, v66\n"
      "v_exp_f32 v67, v67\n"
      "v_exp_f32 v68, v68\n"
      "v_mfma_f32_32x32x4_2b_bf16 v[96:127], v[152:153], v[38:39], 0\n"
      "ds_read_b64 v[152:153], v60 offset:56320\n"
      "v_exp_f32 v69, v69\n"
      "v_exp_f32 v70, v70\n"
      "v_exp_f32 v71, v71\n"
      "v_exp_f32 v72, v72\n"
      "v_exp_f32 v73, v73\n"
      "v_exp_f32 v74, v74\n"
      "v_exp_f32 v75, v75\n"
      "v_exp_f32 v76, v76\n"
      "v_smfmac_f32_32x32x32_bf16 v[40:55], v[128:131], v[64:71], %[ix]\n"
      "ds_read_b128 v[128:131], v61 offset:50688\n"
      "v_exp_f32 v77, v77\n"
      "v_exp_f32 v78, v78\n"
      "v_exp_f32 v79, v79\n"
      "v_exp_f32 v80, v80\n"
      "v_exp_f32 v81, v81\n"
      "v_smfmac_f32_32x32x32_bf16 v[40:55], v[132:135], v[72:79], %[ix]\n"
      "ds_read_b128 v[132:135], v61 offset:51456\n"
      "v_exp_f32 v82, v82\n"
      "v_exp_f32 v83, v83\n"
      "v_exp_f32 v84, v84\n"
      "v_mfma_f32_32x32x16_bf16 v[96:111], v[144:147], v[34:37], v[96:111]\n"
      "ds_read_b128 v[144:147], %[l16] offset:56320\n"
      "v_exp_f32 v85, v85\n"
      "v_exp_f32 v86, v86\n"
      "v_exp_f32 v87, v87\n"
      "v_exp_f32 v88, v88\n"
      "v_mfma_f32_32x32x16_bf16 v[112:127], v[148:151], v[34:37], v[112:127]\n"
      "ds_read_b128 v[148:151], %[l16] offset:57344\n"
      "v_exp_f32 v89, v89\n"
      "v_exp_f32 v90, v90\n"
      "v_exp_f32 v91, v91\n"
      "v_smfmac_f32_32x32x32_bf16 v[40:55], v[136:139], v[80:87], %[ix]\n"
      "ds_read_b128 v[136:139], v61 offset:52224\n"
      "v_exp_f32 v92, v92\n"
      "v_exp_f32 v93, v93\n"
      "v_exp_f32 v94, v94\n"
      "v_exp_f32 v95, v95\n"
      "v_exp_f32 v96, v96\n"
      "v_exp_f32 v97, v97\n"
      "s_waitcnt lgkmcnt(0)\n"
      "v_smfmac_f32_32x32x32_bf16 v[40:55], v[140:143], v[88:95], %[ix]\n"
      "ds_read_b128 v[140:143], v61 offset:52992\n"
      "v_exp_f32 v98, v98\n"
      "v_exp_f32 v99, v99\n"
      "v_exp_f32 v100, v100\n"
      "v_mfma_f32_32x32x4_2b_bf16 v[64:95], v[152:153], v[38:39], 0\n"
      "ds_read_b64 v[152:153], v60 offset:61952\n"
      "v_exp_f32 v101, v101\n"
      "v_exp_f32 v102, v102\n"
      "v_exp_f32 v103, v103\n"
      "v_exp_f32 v104, v104\n"
      "v_exp_f32 v105, v105\n"
      "v_exp_f32 v106, v106\n"
      "v_exp_f32 v107, v107\n"
      "v_exp_f32 v108, v108\n"
      "v_smfmac_f32_32x32x32_bf16 v[40:55], v[128:131], v[96:103], %[ix]\n"
      "ds_read_b128 v[128:131], v61 offset:56320\n"
      "v_exp_f32 v109, v109\n"
      "v_exp_f32 v110, v110\n"
      "v_exp_f32 v111, v111\n"
      "v_exp_f32 v112, v112\n"
      "v_exp_f32 v113, v113\n"
      "v_smfmac_f32_32x32x32_bf16 v[40:55], v[132:135], v[104:111], %[ix]\n"
      "ds_read_b128 v[132:135], v61 offset:57088\n"
      "v_exp_f32 v114, v114\n"
      "v_exp_f32 v115, v115\n"
      "v_exp_f32 v116, v116\n"
      "v_mfma_f32_32x32x16_bf16 v[64:79], v[144:147], v[34:37], v[64:79]\n"
      "ds_read_b128 v[144:147], %[l16] offset:61952\n"
      "v_exp_f32 v117, v117\n"
      "v_exp_f32 v118, v118\n"
      "v_exp_f32 v119, v119\n"
      "v_exp_f32 v120, v120\n"
      "v_mfma_f32_32x32x16_bf16 v[80:95], v[148:151], v[34:37], v[80:95]\n"
      "ds_read_b128 v[148:151], %[l16] offset:62976\n"
      "v_exp_f32 v121, v121\n"
      "v_exp_f32 v122, v122\n"
      "v_exp_f32 v123, v123\n"
      "v_smfmac_f32_32x32x32_bf16 v[40:55], v[136:139], v[112:119], %[ix]\n"
      "ds_read_b128 v[136:139], v61 offset:57856\n"
      "v_exp_f32 v124, v124\n"
      "v_exp_f32 v125, v125\n"
      "v_exp_f32 v126, v126\n"
      "v_exp_f32 v127, v127\n"
      "s_waitcnt vmcnt(0)\n"
      "s_barrier\n"
      "v_exp_f32 v64, v64\n"
      "v_exp_f32 v65, v65\n"
      "s_waitcnt lgkmcnt(0)\n"
      "v_smfmac_f32_32x32x32_bf16 v[40:55], v[140:143], v[120:127], %[ix]\n"
      "ds_read_b128 v[140:143], v61 offset:58624\n"
      "v_exp_f32 v66, v66\n"
      "v_exp_f32 v67, v67\n"
      "v_exp_f32 v68, v68\n"
      "v_mfma_f32_32x32x4_2b_bf16 v[96:127], v[152:153], v[38:39], 0\n"
      "ds_read_b64 v[152:153], v63 offset:0\n"
      "v_exp_f32 v69, v69\n"
      "v_exp_f32 v70, v70\n"
      "v_exp_f32 v71, v71\n"
      "v_exp_f32 v72, v72\n"
      "v_exp_f32 v73, v73\n"
      "v_exp_f32 v74, v74\n"
      "v_exp_f32 v75, v75\n"
      "v_exp_f32 v76, v76\n"
      "v_smfmac_f32_32x32x32_bf16 v[40:55], v[128:131], v[64:71], %[ix]\n"
      "ds_read_b128 v[128:131], v61 offset:61952\n"
      "v_exp_f32 v77, v77\n"
      "v_exp_f32 v78, v78\n"
      "v_exp_f32 v79, v79\n"
      "v_exp_f32 v80, v80\n"
      "v_exp_f32 v81, v81\n"
      "v_smfmac_f32_32x32x32_bf16 v[40:55], v[132:135], v[72:79], %[ix]\n"
      "ds_read_b128 v[132:135], v61 offset:62720\n"
      "v_exp_f32 v82, v82\n"
      "v_exp_f32 v83, v83\n"
      "v_exp_f32 v84, v84\n"
      "v_mfma_f32_32x32x16_bf16 v[96:111], v[144:147], v[34:37], v[96:111]\n"
      "ds_read_b128 v[144:147], v62 offset:0\n"
      "v_exp_f32 v85, v85\n"
      "v_exp_f32 v86, v86\n"
      "v_exp_f32 v87, v87\n"
      "v_exp_f32 v88, v88\n"
      "v_mfma_f32_32x32x16_bf16 v[112:127], v[148:151], v[34:37], v[112:127]\n"
      "ds_read_b128 v[148:151], v62 offset:1024\n"
      "v_exp_f32 v89, v89\n"
      "v_exp_f32 v90, v90\n"
      "v_exp_f32 v91, v91\n"
      "v_smfmac_f32_32x32x32_bf16 v[40:55], v[136:139], v[80:87], %[ix]\n"
      "ds_read_b128 v[136:139], v61 offset:63488\n"
      "s_mov_b32 m0, %[m0c]\n"
      "v_add_u32 %[g], %[m0c], %[l16]\n"
      "v_add_u32 %[g], 135168, %[g]\n"
      "global_load_lds_dwordx4 %[g], %[kv] offset:0 sc1\n"
      "v_exp_f32 v92, v92\n"
      "v_exp_f32 v93, v93\n"
      "v_exp_f32 v94, v94\n"
      "v_exp_f32 v95, v95\n"
      "v_exp_f32 v96, v96\n"
      "v_exp_f32 v97, v97\n"
      "s_waitcnt lgkmcnt(0)\n"
      "v_smfmac_f32_32x32x32_bf16 v[40:55], v[140:143], v[88:95], %[ix]\n"
      "ds_read_b128 v[140:143], v61 offset:64256\n"
      "v_exp_f32 v98, v98\n"
      "v_exp_f32 v99, v99\n"
      "v_exp_f32 v100, v100\n"
      "v_mfma_f32_32x32x4_2b_bf16 v[64:95], v[152:153], v[38:39], 0\n"
      "ds_read_b64 v[152:153], v63 offset:5632\n"
      "v_exp_f32 v101, v101\n"
      "v_exp_f32 v102, v102\n"
      "v_exp_f32 v103, v103\n"
      "v_exp_f32 v104, v104\n"
      "v_exp_f32 v105, v105\n"
      "v_exp_f32 v106, v106\n"
      "v_exp_f32 v107, v107\n"
      "v_exp_f32 v108, v108\n"
      "v_smfmac_f32_32x32x32_bf16 v[40:55], v[128:131], v[96:103], %[ix]\n"
      "ds_read_b128 v[128:131], %[av] offset:0\n"
      "v_exp_f32 v109, v109\n"
      "v_exp_f32 v110, v110\n"
      "v_exp_f32 v111, v111\n"
      "v_exp_f32 v112, v112\n"
      "v_exp_f32 v113, v113\n"
      "v_smfmac_f32_32x32x32_bf16 v[40:55], v[132:135], v[104:111], %[ix]\n"
      "ds_read_b128 v[132:135], %[av] offset:768\n"
      "v_exp_f32 v114, v114\n"
      "v_exp_f32 v115, v115\n"
      "v_exp_f32 v116, v116\n"
      "v_mfma_f32_32x32x16_bf16 v[64:79], v[144:147], v[34:37], v[64:79]\n"
      "ds_read_b128 v[144:147], v62 offset:5632\n"
      "v_exp_f32 v117, v117\n"
      "v_exp_f32 v118, v118\n"
      "v_exp_f32 v119, v119\n"
      "v_exp_f32 v120, v120\n"
      "v_mfma_f32_32x32x16_bf16 v[80:95], v[148:151], v[34:37], v[80:95]\n"
      "ds_read_b128 v[148:151], v62 offset:6656\n"
      "v_exp_f32 v121, v121\n"
      "v_exp_f32 v122, v122\n"
      "v_exp_f32 v123, v123\n"
      "v_smfmac_f32_32x32x32_bf16 v[40:55], v[136:139], v[112:119], %[ix]\n"
      "ds_read_b128 v[136:139], %[av] offset:1536\n"
      "global_load_lds_dwordx4 %[g], %[kv] offset:1024 sc1\n"
      "v_exp_f32 v124, v124\n"
      "v_exp_f32 v125, v125\n"
      "v_exp_f32 v126, v126\n"
      "v_exp_f32 v127, v127\n"
      "v_exp_f32 v64, v64\n"
      "v_exp_f32 v65, v65\n"
      "s_waitcnt lgkmcnt(0)\n"
      "v_smfmac_f32_32x32x32_bf16 v[40:55], v[140:143], v[120:127], %[ix]\n"
      "ds_read_b128 v[140:143], %[av] offset:2304\n"
      "v_exp_f32 v66, v66\n"
      "v_exp_f32 v67, v67\n"
      "v_exp_f32 v68, v68\n"
      "v_mfma_f32_32x32x4_2b_bf16 v[96:127], v[152:153], v[38:39], 0\n"
      "ds_read_b64 v[152:153], v63 offset:11264\n"
      "v_exp_f32 v69, v69\n"
      "v_exp_f32 v70, v70\n"
      "v_exp_f32 v71, v71\n"
      "v_exp_f32 v72, v72\n"
      "v_exp_f32 v73, v73\n"
      "v_exp_f32 v74, v74\n"
      "v_exp_f32 v75, v75\n"
      "v_exp_f32 v76, v76\n"
      "v_smfmac_f32_32x32x32_bf16 v[40:55], v[128:131], v[64:71], %[ix]\n"
      "ds_read_b128 v[128:131], %[av] offset:5632\n"
      "v_exp_f32 v77, v77\n"
      "v_exp_f32 v78, v78\n"
      "v_exp_f32 v79, v79\n"
      "v_exp_f32 v80, v80\n"
      "v_exp_f32 v81, v81\n"
      "v_smfmac_f32_32x32x32_bf16 v[40:55], v[132:135], v[72:79], %[ix]\n"
      "ds_read_b128 v[132:135], %[av] offset:6400\n"
      "v_exp_f32 v82, v82\n"
      "v_exp_f32 v83, v83\n"
      "v_exp_f32 v84, v84\n"
      "v_mfma_f32_32x32x16_bf16 v[96:111], v[144:147], v[34:37], v[96:111]\n"
      "ds_read_b128 v[144:147], v62 offset:11264\n"
      "v_exp_f32 v85, v85\n"
      "v_exp_f32 v86, v86\n"
      "v_exp_f32 v87, v87\n"
      "v_exp_f32 v88, v88\n"
      "v_mfma_f32_32x32x16_bf16 v[112:127], v[148:151], v[34:37], v[112:127]\n"
      "ds_read_b128 v[148:151], v62 offset:12288\n"
      "v_exp_f32 v89, v89\n"
      "v_exp_f32 v90, v90\n"
      "v_exp_f32 v91, v91\n"
      "v_smfmac_f32_32x32x32_bf16 v[40:55], v[136:139], v[80:87], %[ix]\n"
      "ds_read_b128 v[136:139], %[av] offset:7168\n"
      "global_load_lds_dwordx4 %[g], %[kv] offset:2048 sc1\n"
      "v_exp_f32 v92, v92\n"
      "v_exp_f32 v93, v93\n"
      "v_exp_f32 v94, v94\n"
      "v_exp_f32 v95, v95\n"
      "v_exp_f32 v96, v96\n"
      "v_exp_f32 v97, v97\n"
      "s_waitcnt lgkmcnt(0)\n"
      "v_smfmac_f32_32x32x32_bf16 v[40:55], v[140:143], v[88:95], %[ix]\n"
      "ds_read_b128 v[140:143], %[av] offset:7936\n"
      "v_exp_f32 v98, v98\n"
      "v_exp_f32 v99, v99\n"
      "v_exp_f32 v100, v100\n"
      "v_mfma_f32_32x32x4_2b_bf16 v[64:95], v[152:153], v[38:39], 0\n"
      "ds_read_b64 v[152:153], v63 offset:16896\n"
      "v_exp_f32 v101, v101\n"
      "v_exp_f32 v102, v102\n"
      "v_exp_f32 v103, v103\n"
      "v_exp_f32 v104, v104\n"
      "v_exp_f32 v105, v105\n"
      "v_exp_f32 v106, v106\n"
      "v_exp_f32 v107, v107\n"
      "v_exp_f32 v108, v108\n"
      "v_smfmac_f32_32x32x32_bf16 v[40:55], v[128:131], v[96:103], %[ix]\n"
      "ds_read_b128 v[128:131], %[av] offset:11264\n"
      "v_exp_f32 v109, v109\n"
      "v_exp_f32 v110, v110\n"
      "v_exp_f32 v111, v111\n"
      "v_exp_f32 v112, v112\n"
      "v_exp_f32 v113, v113\n"
      "v_smfmac_f32_32x32x32_bf16 v[40:55], v[132:135], v[104:111], %[ix]\n"
      "ds_read_b128 v[132:135], %[av] offset:12032\n"
      "v_exp_f32 v114, v114\n"
      "v_exp_f32 v115, v115\n"
      "v_exp_f32 v116, v116\n"
      "v_mfma_f32_32x32x16_bf16 v[64:79], v[144:147], v[34:37], v[64:79]\n"
      "ds_read_b128 v[144:147], v62 offset:16896\n"
      "v_exp_f32 v117, v117\n"
      "v_exp_f32 v118, v118\n"
      "v_exp_f32 v119, v119\n"
      "v_exp_f32 v120, v120\n"
      "v_mfma_f32_32x32x16_bf16 v[80:95], v[148:151], v[34:37], v[80:95]\n"
      "ds_read_b128 v[148:151], v62 offset:17920\n"
      "v_exp_f32 v121, v121\n"
      "v_exp_f32 v122, v122\n"
      "v_exp_f32 v123, v123\n"
      "v_smfmac_f32_32x32x32_bf16 v[40:55], v[136:139], v[112:119], %[ix]\n"
      "ds_read_b128 v[136:139], %[av] offset:12800\n"
      "global_load_lds_dwordx4 %[g], %[kv] offset:3072 sc1\n"
      "v_exp_f32 v124, v124\n"
      "v_exp_f32 v125, v125\n"
      "v_exp_f32 v126, v126\n"
      "v_exp_f32 v127, v127\n"
      "v_exp_f32 v64, v64\n"
      "v_exp_f32 v65, v65\n"
      "s_waitcnt lgkmcnt(0)\n"
      "v_smfmac_f32_32x32x32_bf16 v[40:55], v[140:143], v[120:127], %[ix]\n"
      "ds_read_b128 v[140:143], %[av] offset:13568\n"
      "v_exp_f32 v66, v66\n"
      "v_exp_f32 v67, v67\n"
      "v_exp_f32 v68, v68\n"
      "v_mfma_f32_32x32x4_2b_bf16 v[96:127], v[152:153], v[38:39], 0\n"
      "ds_read_b64 v[152:153], v63 offset:22528\n"
      "v_exp_f32 v69, v69\n"
      "v_exp_f32 v70, v70\n"
      "v_exp_f32 v71, v71\n"
      "v_exp_f32 v72, v72\n"
      "v_exp_f32 v73, v73\n"
      "v_exp_f32 v74, v74\n"
      "v_exp_f32 v75, v75\n"
      "v_exp_f32 v76, v76\n"
      "v_smfmac_f32_32x32x32_bf16 v[40:55], v[128:131], v[64:71], %[ix]\n"
      "ds_read_b128 v[128:131], %[av] offset:16896\n"
      "v_exp_f32 v77, v77\n"
      "v_exp_f32 v78, v78\n"
      "v_exp_f32 v79, v79\n"
      "v_exp_f32 v80, v80\n"
      "v_exp_f32 v81, v81\n"
      "v_smfmac_f32_32x32x32_bf16 v[40:55], v[132:135], v[72:79], %[ix]\n"
      "ds_read_b128 v[132:135], %[av] offset:17664\n"
      "v_exp_f32 v82, v82\n"
      "v_exp_f32 v83, v83\n"
      "v_exp_f32 v84, v84\n"
      "v_mfma_f32_32x32x16_bf16 v[96:111], v[144:147], v[34:37], v[96:111]\n"
      "ds_read_b128 v[144:147], v62 offset:22528\n"
      "v_exp_f32 v85, v85\n"
      "v_exp_f32 v86, v86\n"
      "v_exp_f32 v87, v87\n"
      "v_exp_f32 v88, v88\n"
      "v_mfma_f32_32x32x16_bf16 v[112:127], v[148:151], v[34:37], v[112:127]\n"
      "ds_read_b128 v[148:151], v62 offset:23552\n"
      "v_exp_f32 v89, v89\n"
      "v_exp_f32 v90, v90\n"
      "v_exp_f32 v91, v91\n"
      "v_smfmac_f32_32x32x32_bf16 v[40:55], v[136:139], v[80:87], %[ix]\n"
      "ds_read_b128 v[136:139], %[av] offset:18432\n"
      "s_add_u32 s44, %[m0c], 0x1000\n"
      "s_mov_b32 m0, s44\n"
      "v_add_u32 %[g], 0x1000, %[g]\n"
      "global_load_lds_dwordx4 %[g], %[kv] offset:0 sc1\n"
      "v_exp_f32 v92, v92\n"
      "v_exp_f32 v93, v93\n"
      "v_exp_f32 v94, v94\n"
      "v_exp_f32 v95, v95\n"
      "v_exp_f32 v96, v96\n"
      "v_exp_f32 v97, v97\n"
      "s_waitcnt lgkmcnt(0)\n"
      "v_smfmac_f32_32x32x32_bf16 v[40:55], v[140:143], v[88:95], %[ix]\n"
      "ds_read_b128 v[140:143], %[av] offset:19200\n"
      "v_exp_f32 v98, v98\n"
      "v_exp_f32 v99, v99\n"
      "v_exp_f32 v100, v100\n"
      "v_mfma_f32_32x32x4_2b_bf16 v[64:95], v[152:153], v[38:39], 0\n"
      "ds_read_b64 v[152:153], v63 offset:28160\n"
      "v_exp_f32 v101, v101\n"
      "v_exp_f32 v102, v102\n"
      "v_exp_f32 v103, v103\n"
      "v_exp_f32 v104, v104\n"
      "v_exp_f32 v105, v105\n"
      "v_exp_f32 v106, v106\n"
      "v_exp_f32 v107, v107\n"
      "v_exp_f32 v108, v108\n"
      "v_smfmac_f32_32x32x32_bf16 v[40:55], v[128:131], v[96:103], %[ix]\n"
      "ds_read_b128 v[128:131], %[av] offset:22528\n"
      "v_exp_f32 v109, v109\n"
      "v_exp_f32 v110, v110\n"
      "v_exp_f32 v111, v111\n"
      "v_exp_f32 v112, v112\n"
      "v_exp_f32 v113, v113\n"
      "v_smfmac_f32_32x32x32_bf16 v[40:55], v[132:135], v[104:111], %[ix]\n"
      "ds_read_b128 v[132:135], %[av] offset:23296\n"
      "v_exp_f32 v114, v114\n"
      "v_exp_f32 v115, v115\n"
      "v_exp_f32 v116, v116\n"
      "v_mfma_f32_32x32x16_bf16 v[64:79], v[144:147], v[34:37], v[64:79]\n"
      "ds_read_b128 v[144:147], v62 offset:28160\n"
      "v_exp_f32 v117, v117\n"
      "v_exp_f32 v118, v118\n"
      "v_exp_f32 v119, v119\n"
      "v_exp_f32 v120, v120\n"
      "v_mfma_f32_32x32x16_bf16 v[80:95], v[148:151], v[34:37], v[80:95]\n"
      "ds_read_b128 v[148:151], v62 offset:29184\n"
      "v_exp_f32 v121, v121\n"
      "v_exp_f32 v122, v122\n"
      "v_exp_f32 v123, v123\n"
      "v_smfmac_f32_32x32x32_bf16 v[40:55], v[136:139], v[112:119], %[ix]\n"
      "ds_read_b128 v[136:139], %[av] offset:24064\n"
      "global_load_lds_dwordx4 %[g], %[kv] offset:1024 sc1\n"
      "v_exp_f32 v124, v124\n"
      "v_exp_f32 v125, v125\n"
      "v_exp_f32 v126, v126\n"
      "v_exp_f32 v127, v127\n"
      "v_exp_f32 v64, v64\n"
      "v_exp_f32 v65, v65\n"
      "s_waitcnt lgkmcnt(0)\n"
      "v_smfmac_f32_32x32x32_bf16 v[40:55], v[140:143], v[120:127], %[ix]\n"
      "ds_read_b128 v[140:143], %[av] offset:24832\n"
      "v_exp_f32 v66, v66\n"
      "v_exp_f32 v67, v67\n"
      "v_exp_f32 v68, v68\n"
      "v_mfma_f32_32x32x4_2b_bf16 v[96:127], v[152:153], v[38:39], 0\n"
      "ds_read_b64 v[152:153], v63 offset:33792\n"
      "v_exp_f32 v69, v69\n"
      "v_exp_f32 v70, v70\n"
      "v_exp_f32 v71, v71\n"
      "v_exp_f32 v72, v72\n"
      "v_exp_f32 v73, v73\n"
      "v_exp_f32 v74, v74\n"
      "v_exp_f32 v75, v75\n"
      "v_exp_f32 v76, v76\n"
      "v_smfmac_f32_32x32x32_bf16 v[40:55], v[128:131], v[64:71], %[ix]\n"
      "ds_read_b128 v[128:131], %[av] offset:28160\n"
      "v_exp_f32 v77, v77\n"
      "v_exp_f32 v78, v78\n"
      "v_exp_f32 v79, v79\n"
      "v_exp_f32 v80, v80\n"
      "v_exp_f32 v81, v81\n"
      "v_smfmac_f32_32x32x32_bf16 v[40:55], v[132:135], v[72:79], %[ix]\n"
      "ds_read_b128 v[132:135], %[av] offset:28928\n"
      "v_exp_f32 v82, v82\n"
      "v_exp_f32 v83, v83\n"
      "v_exp_f32 v84, v84\n"
      "v_mfma_f32_32x32x16_bf16 v[96:111], v[144:147], v[34:37], v[96:111]\n"
      "ds_read_b128 v[144:147], v62 offset:33792\n"
      "v_exp_f32 v85, v85\n"
      "v_exp_f32 v86, v86\n"
      "v_exp_f32 v87, v87\n"
      "v_exp_f32 v88, v88\n"
      "v_mfma_f32_32x32x16_bf16 v[112:127], v[148:151], v[34:37], v[112:127]\n"
      "ds_read_b128 v[148:151], v62 offset:34816\n"
      "v_exp_f32 v89, v89\n"
      "v_exp_f32 v90, v90\n"
      "v_exp_f32 v91, v91\n"
      "v_smfmac_f32_32x32x32_bf16 v[40:55], v[136:139], v[80:87], %[ix]\n"
      "ds_read_b128 v[136:139], %[av] offset:29696\n"
      "v_exp_f32 v92, v92\n"
      "v_exp_f32 v93, v93\n"
      "v_exp_f32 v94, v94\n"
      "v_exp_f32 v95, v95\n"
      "v_exp_f32 v96, v96\n"
      "v_exp_f32 v97, v97\n"
      "s_waitcnt lgkmcnt(0)\n"
      "v_smfmac_f32_32x32x32_bf16 v[40:55], v[140:143], v[88:95], %[ix]\n"
      "ds_read_b128 v[140:143], %[av] offset:30464\n"
      "v_exp_f32 v98, v98\n"
      "v_exp_f32 v99, v99\n"
      "v_exp_f32 v100, v100\n"
      "v_mfma_f32_32x32x4_2b_bf16 v[64:95], v[152:153], v[38:39], 0\n"
      "ds_read_b64 v[152:153], v63 offset:39424\n"
      "v_exp_f32 v101, v101\n"
      "v_exp_f32 v102, v102\n"
      "v_exp_f32 v103, v103\n"
      "v_exp_f32 v104, v104\n"
      "v_exp_f32 v105, v105\n"
      "v_exp_f32 v106, v106\n"
      "v_exp_f32 v107, v107\n"
      "v_exp_f32 v108, v108\n"
      "v_smfmac_f32_32x32x32_bf16 v[40:55], v[128:131], v[96:103], %[ix]\n"
      "ds_read_b128 v[128:131], %[av] offset:33792\n"
      "v_exp_f32 v109, v109\n"
      "v_exp_f32 v110, v110\n"
      "v_exp_f32 v111, v111\n"
      "v_exp_f32 v112, v112\n"
      "v_exp_f32 v113, v113\n"
      "v_smfmac_f32_32x32x32_bf16 v[40:55], v[132:135], v[104:111], %[ix]\n"
      "ds_read_b128 v[132:135], %[av] offset:34560\n"
      "v_exp_f32 v114, v114\n"
      "v_exp_f32 v115, v115\n"
      "v_exp_f32 v116, v116\n"
      "v_mfma_f32_32x32x16_bf16 v[64:79], v[144:147], v[34:37], v[64:79]\n"
      "ds_read_b128 v[144:147], v62 offset:39424\n"
      "v_exp_f32 v117, v117\n"
      "v_exp_f32 v118, v118\n"
      "v_exp_f32 v119, v119\n"
      "v_exp_f32 v120, v120\n"
      "v_mfma_f32_32x32x16_bf16 v[80:95], v[148:151], v[34:37], v[80:95]\n"
      "ds_read_b128 v[148:151], v62 offset:40448\n"
      "v_exp_f32 v121, v121\n"
      "v_exp_f32 v122, v122\n"
      "v_exp_f32 v123, v123\n"
      "v_smfmac_f32_32x32x32_bf16 v[40:55], v[136:139], v[112:119], %[ix]\n"
      "ds_read_b128 v[136:139], %[av] offset:35328\n"
      "v_exp_f32 v124, v124\n"
      "v_exp_f32 v125, v125\n"
      "v_exp_f32 v126, v126\n"
      "v_exp_f32 v127, v127\n"
      "v_exp_f32 v64, v64\n"
      "v_exp_f32 v65, v65\n"
      "s_waitcnt lgkmcnt(0)\n"
      "v_smfmac_f32_32x32x32_bf16 v[40:55], v[140:143], v[120:127], %[ix]\n"
      "ds_read_b128 v[140:143], %[av] offset:36096\n"
      "v_exp_f32 v66, v66\n"
      "v_exp_f32 v67, v67\n"
      "v_exp_f32 v68, v68\n"
      "v_mfma_f32_32x32x4_2b_bf16 v[96:127], v[152:153], v[38:39], 0\n"
      "ds_read_b64 v[152:153], v63 offset:45056\n"
      "v_exp_f32 v69, v69\n"
      "v_exp_f32 v70, v70\n"
      "v_exp_f32 v71, v71\n"
      "v_exp_f32 v72, v72\n"
      "v_exp_f32 v73, v73\n"
      "v_exp_f32 v74, v74\n"
      "v_exp_f32 v75, v75\n"
      "v_exp_f32 v76, v76\n"
      "v_smfmac_f32_32x32x32_bf16 v[40:55], v[128:131], v[64:71], %[ix]\n"
      "ds_read_b128 v[128:131], %[av] offset:39424\n"
      "v_exp_f32 v77, v77\n"
      "v_exp_f32 v78, v78\n"
      "v_exp_f32 v79, v79\n"
      "v_exp_f32 v80, v80\n"
      "v_exp_f32 v81, v81\n"
      "v_smfmac_f32_32x32x32_bf16 v[40:55], v[132:135], v[72:79], %[ix]\n"
      "ds_read_b128 v[132:135], %[av] offset:40192\n"
      "v_exp_f32 v82, v82\n"
      "v_exp_f32 v83, v83\n"
      "v_exp_f32 v84, v84\n"
      "v_mfma_f32_32x32x16_bf16 v[96:111], v[144:147], v[34:37], v[96:111]\n"
      "ds_read_b128 v[144:147], v62 offset:45056\n"
      "v_exp_f32 v85, v85\n"
      "v_exp_f32 v86, v86\n"
      "v_exp_f32 v87, v87\n"
      "v_exp_f32 v88, v88\n"
      "v_mfma_f32_32x32x16_bf16 v[112:127], v[148:151], v[34:37], v[112:127]\n"
      "ds_read_b128 v[148:151], v62 offset:46080\n"
      "v_exp_f32 v89, v89\n"
      "v_exp_f32 v90, v90\n"
      "v_exp_f32 v91, v91\n"
      "v_smfmac_f32_32x32x32_bf16 v[40:55], v[136:139], v[80:87], %[ix]\n"
      "ds_read_b128 v[136:139], %[av] offset:40960\n"
      "v_exp_f32 v92, v92\n"
      "v_exp_f32 v93, v93\n"
      "v_exp_f32 v94, v94\n"
      "v_exp_f32 v95, v95\n"
      "v_exp_f32 v96, v96\n"
      "v_exp_f32 v97, v97\n"
      "s_waitcnt lgkmcnt(0)\n"
      "v_smfmac_f32_32x32x32_bf16 v[40:55], v[140:143], v[88:95], %[ix]\n"
      "ds_read_b128 v[140:143], %[av] offset:41728\n"
      "v_exp_f32 v98, v98\n"
      "v_exp_f32 v99, v99\n"
      "v_exp_f32 v100, v100\n"
      "v_mfma_f32_32x32x4_2b_bf16 v[64:95], v[152:153], v[38:39], 0\n"
      "ds_read_b64 v[152:153], v63 offset:50688\n"
      "v_exp_f32 v101, v101\n"
      "v_exp_f32 v102, v102\n"
      "v_exp_f32 v103, v103\n"
      "v_exp_f32 v104, v104\n"
      "v_exp_f32 v105, v105\n"
      "v_exp_f32 v106, v106\n"
      "v_exp_f32 v107, v107\n"
      "v_exp_f32 v108, v108\n"
      "v_smfmac_f32_32x32x32_bf16 v[40:55], v[128:131], v[96:103], %[ix]\n"
      "ds_read_b128 v[128:131], %[av] offset:45056\n"
      "v_exp_f32 v109, v109\n"
      "v_exp_f32 v110, v110\n"
      "v_exp_f32 v111, v111\n"
      "v_exp_f32 v112, v112\n"
      "v_exp_f32 v113, v113\n"
      "v_smfmac_f32_32x32x32_bf16 v[40:55], v[132:135], v[104:111], %[ix]\n"
      "ds_read_b128 v[132:135], %[av] offset:45824\n"
      "v_exp_f32 v114, v114\n"
      "v_exp_f32 v115, v115\n"
      "v_exp_f32 v116, v116\n"
      "v_mfma_f32_32x32x16_bf16 v[64:79], v[144:147], v[34:37], v[64:79]\n"
      "ds_read_b128 v[144:147], v62 offset:50688\n"
      "v_exp_f32 v117, v117\n"
      "v_exp_f32 v118, v118\n"
      "v_exp_f32 v119, v119\n"
      "v_exp_f32 v120, v120\n"
      "v_mfma_f32_32x32x16_bf16 v[80:95], v[148:151], v[34:37], v[80:95]\n"
      "ds_read_b128 v[148:151], v62 offset:51712\n"
      "v_exp_f32 v121, v121\n"
      "v_exp_f32 v122, v122\n"
      "v_exp_f32 v123, v123\n"
      "v_smfmac_f32_32x32x32_bf16 v[40:55], v[136:139], v[112:119], %[ix]\n"
      "ds_read_b128 v[136:139], %[av] offset:46592\n"
      "v_exp_f32 v124, v124\n"
      "v_exp_f32 v125, v125\n"
      "v_exp_f32 v126, v126\n"
      "v_exp_f32 v127, v127\n"
      "v_exp_f32 v64, v64\n"
      "v_exp_f32 v65, v65\n"
      "s_waitcnt lgkmcnt(0)\n"
      "v_smfmac_f32_32x32x32_bf16 v[40:55], v[140:143], v[120:127], %[ix]\n"
      "ds_read_b128 v[140:143], %[av] offset:47360\n"
      "v_exp_f32 v66, v66\n"
      "v_exp_f32 v67, v67\n"
      "v_exp_f32 v68, v68\n"
      "v_mfma_f32_32x32x4_2b_bf16 v[96:127], v[152:153], v[38:39], 0\n"
      "ds_read_b64 v[152:153], v63 offset:56320\n"
      "v_exp_f32 v69, v69\n"
      "v_exp_f32 v70, v70\n"
      "v_exp_f32 v71, v71\n"
      "v_exp_f32 v72, v72\n"
      "v_exp_f32 v73, v73\n"
      "v_exp_f32 v74, v74\n"
      "v_exp_f32 v75, v75\n"
      "v_exp_f32 v76, v76\n"
      "v_smfmac_f32_32x32x32_bf16 v[40:55], v[128:131], v[64:71], %[ix]\n"
      "ds_read_b128 v[128:131], %[av] offset:50688\n"
      "v_exp_f32 v77, v77\n"
      "v_exp_f32 v78, v78\n"
      "v_exp_f32 v79, v79\n"
      "v_exp_f32 v80, v80\n"
      "v_exp_f32 v81, v81\n"
      "v_smfmac_f32_32x32x32_bf16 v[40:55], v[132:135], v[72:79], %[ix]\n"
      "ds_read_b128 v[132:135], %[av] offset:51456\n"
      "v_exp_f32 v82, v82\n"
      "v_exp_f32 v83, v83\n"
      "v_exp_f32 v84, v84\n"
      "v_mfma_f32_32x32x16_bf16 v[96:111], v[144:147], v[34:37], v[96:111]\n"
      "ds_read_b128 v[144:147], v62 offset:56320\n"
      "v_exp_f32 v85, v85\n"
      "v_exp_f32 v86, v86\n"
      "v_exp_f32 v87, v87\n"
      "v_exp_f32 v88, v88\n"
      "v_mfma_f32_32x32x16_bf16 v[112:127], v[148:151], v[34:37], v[112:127]\n"
      "ds_read_b128 v[148:151], v62 offset:57344\n"
      "v_exp_f32 v89, v89\n"
      "v_exp_f32 v90, v90\n"
      "v_exp_f32 v91, v91\n"
      "v_smfmac_f32_32x32x32_bf16 v[40:55], v[136:139], v[80:87], %[ix]\n"
      "ds_read_b128 v[136:139], %[av] offset:52224\n"
      "v_exp_f32 v92, v92\n"
      "v_exp_f32 v93, v93\n"
      "v_exp_f32 v94, v94\n"
      "v_exp_f32 v95, v95\n"
      "v_exp_f32 v96, v96\n"
      "v_exp_f32 v97, v97\n"
      "s_waitcnt lgkmcnt(0)\n"
      "v_smfmac_f32_32x32x32_bf16 v[40:55], v[140:143], v[88:95], %[ix]\n"
      "ds_read_b128 v[140:143], %[av] offset:52992\n"
      "v_exp_f32 v98, v98\n"
      "v_exp_f32 v99, v99\n"
      "v_exp_f32 v100, v100\n"
      "v_mfma_f32_32x32x4_2b_bf16 v[64:95], v[152:153], v[38:39], 0\n"
      "ds_read_b64 v[152:153], v63 offset:61952\n"
      "v_exp_f32 v101, v101\n"
      "v_exp_f32 v102, v102\n"
      "v_exp_f32 v103, v103\n"
      "v_exp_f32 v104, v104\n"
      "v_exp_f32 v105, v105\n"
      "v_exp_f32 v106, v106\n"
      "v_exp_f32 v107, v107\n"
      "v_exp_f32 v108, v108\n"
      "v_smfmac_f32_32x32x32_bf16 v[40:55], v[128:131], v[96:103], %[ix]\n"
      "ds_read_b128 v[128:131], %[av] offset:56320\n"
      "v_exp_f32 v109, v109\n"
      "v_exp_f32 v110, v110\n"
      "v_exp_f32 v111, v111\n"
      "v_exp_f32 v112, v112\n"
      "v_exp_f32 v113, v113\n"
      "v_smfmac_f32_32x32x32_bf16 v[40:55], v[132:135], v[104:111], %[ix]\n"
      "ds_read_b128 v[132:135], %[av] offset:57088\n"
      "v_exp_f32 v114, v114\n"
      "v_exp_f32 v115, v115\n"
      "v_exp_f32 v116, v116\n"
      "v_mfma_f32_32x32x16_bf16 v[64:79], v[144:147], v[34:37], v[64:79]\n"
      "ds_read_b128 v[144:147], v62 offset:61952\n"
      "v_exp_f32 v117, v117\n"
      "v_exp_f32 v118, v118\n"
      "v_exp_f32 v119, v119\n"
      "v_exp_f32 v120, v120\n"
      "v_mfma_f32_32x32x16_bf16 v[80:95], v[148:151], v[34:37], v[80:95]\n"
      "ds_read_b128 v[148:151], v62 offset:62976\n"
      "v_exp_f32 v121, v121\n"
      "v_exp_f32 v122, v122\n"
      "v_exp_f32 v123, v123\n"
      "v_smfmac_f32_32x32x32_bf16 v[40:55], v[136:139], v[112:119], %[ix]\n"
      "ds_read_b128 v[136:139], %[av] offset:57856\n"
      "v_exp_f32 v124, v124\n"
      "v_exp_f32 v125, v125\n"
      "v_exp_f32 v126, v126\n"
      "v_exp_f32 v127, v127\n"
      "s_waitcnt vmcnt(0)\n"
      "s_barrier\n"
      "v_exp_f32 v64, v64\n"
      "v_exp_f32 v65, v65\n"
      "s_waitcnt lgkmcnt(0)\n"
      "v_smfmac_f32_32x32x32_bf16 v[40:55], v[140:143], v[120:127], %[ix]\n"
      "ds_read_b128 v[140:143], %[av] offset:58624\n"
      "v_exp_f32 v66, v66\n"
      "v_exp_f32 v67, v67\n"
      "v_exp_f32 v68, v68\n"
      "v_mfma_f32_32x32x4_2b_bf16 v[96:127], v[152:153], v[38:39], 0\n"
      "ds_read_b64 v[152:153], v60 offset:0\n"
      "v_exp_f32 v69, v69\n"
      "v_exp_f32 v70, v70\n"
      "v_exp_f32 v71, v71\n"
      "v_exp_f32 v72, v72\n"
      "v_exp_f32 v73, v73\n"
      "v_exp_f32 v74, v74\n"
      "v_exp_f32 v75, v75\n"
      "v_exp_f32 v76, v76\n"
      "v_smfmac_f32_32x32x32_bf16 v[40:55], v[128:131], v[64:71], %[ix]\n"
      "ds_read_b128 v[128:131], %[av] offset:61952\n"
      "s_mov_b64 s[46:47], %[wf]\n"
      "global_load_dwordx4 v[154:157], %[l16], s[46:47] offset:0 sc1\n"
      "v_exp_f32 v77, v77\n"
      "v_exp_f32 v78, v78\n"
      "v_exp_f32 v79, v79\n"
      "v_exp_f32 v80, v80\n"
      "v_exp_f32 v81, v81\n"
      "v_smfmac_f32_32x32x32_bf16 v[40:55], v[132:135], v[72:79], %[ix]\n"
      "ds_read_b128 v[132:135], %[av] offset:62720\n"
      "global_load_dwordx4 v[158:161], %[l16], s[46:47] offset:1024 sc1\n"
      "v_exp_f32 v82, v82\n"
      "v_exp_f32 v83, v83\n"
      "v_exp_f32 v84, v84\n"
      "v_mfma_f32_32x32x16_bf16 v[96:111], v[144:147], v[34:37], v[96:111]\n"
      "ds_read_b128 v[144:147], %[l16] offset:0\n"
      "v_exp_f32 v85, v85\n"
      "v_exp_f32 v86, v86\n"
      "v_exp_f32 v87, v87\n"
      "v_exp_f32 v88, v88\n"
      "v_mfma_f32_32x32x16_bf16 v[112:127], v[148:151], v[34:37], v[112:127]\n"
      "ds_read_b128 v[148:151], %[l16] offset:1024\n"
      "v_exp_f32 v89, v89\n"
      "v_exp_f32 v90, v90\n"
      "v_exp_f32 v91, v91\n"
      "v_smfmac_f32_32x32x32_bf16 v[40:55], v[136:139], v[80:87], %[ix]\n"
      "ds_read_b128 v[136:139], %[av] offset:63488\n"
      "global_load_dwordx4 v[162:165], %[l16], s[46:47] offset:2048 sc1\n"
      "v_exp_f32 v92, v92\n"
      "v_exp_f32 v93, v93\n"
      "v_exp_f32 v94, v94\n"
      "v_exp_f32 v95, v95\n"
      "v_exp_f32 v96, v96\n"
      "v_exp_f32 v97, v97\n"
      "s_waitcnt lgkmcnt(0)\n"
      "v_smfmac_f32_32x32x32_bf16 v[40:55], v[140:143], v[88:95], %[ix]\n"
      "ds_read_b128 v[140:143], %[av] offset:64256\n"
      "v_exp_f32 v98, v98\n"
      "v_exp_f32 v99, v99\n"
      "v_exp_f32 v100, v100\n"
      "v_mfma_f32_32x32x4_2b_bf16 v[64:95], v[152:153], v[38:39], 0\n"
      "ds_read_b64 v[152:153], v60 offset:5632\n"
      "v_exp_f32 v101, v101\n"
      "v_exp_f32 v102, v102\n"
      "v_exp_f32 v103, v103\n"
      "v_exp_f32 v104, v104\n"
      "v_exp_f32 v105, v105\n"
      "v_exp_f32 v106, v106\n"
      "v_exp_f32 v107, v107\n"
      "v_exp_f32 v108, v108\n"
      "v_smfmac_f32_32x32x32_bf16 v[40:55], v[128:131], v[96:103], %[ix]\n"
      "ds_read_b128 v[128:131], v61 offset:0\n"
      "global_load_dwordx4 v[166:169], %[l16], s[46:47] offset:3072 sc1\n"
      "v_exp_f32 v109, v109\n"
      "v_exp_f32 v110, v110\n"
      "v_exp_f32 v111, v111\n"
      "v_exp_f32 v112, v112\n"
      "v_exp_f32 v113, v113\n"
      "v_smfmac_f32_32x32x32_bf16 v[40:55], v[132:135], v[104:111], %[ix]\n"
      "ds_read_b128 v[132:135], v61 offset:768\n"
      "s_add_u32 s46, s46, 0x1000\n"
      "s_addc_u32 s47, s47, 0\n"
      "global_load_dwordx4 v[170:173], %[l16], s[46:47] offset:0 sc1\n"
      "v_exp_f32 v114, v114\n"
      "v_exp_f32 v115, v115\n"
      "v_exp_f32 v116, v116\n"
      "v_mfma_f32_32x32x16_bf16 v[64:79], v[144:147], v[34:37], v[64:79]\n"
      "ds_read_b128 v[144:147], %[l16] offset:5632\n"
      "v_exp_f32 v117, v117\n"
      "v_exp_f32 v118, v118\n"
      "v_exp_f32 v119, v119\n"
      "v_exp_f32 v120, v120\n"
      "v_mfma_f32_32x32x16_bf16 v[80:95], v[148:151], v[34:37], v[80:95]\n"
      "ds_read_b128 v[148:151], %[l16] offset:6656\n"
      "v_exp_f32 v121, v121\n"
      "v_exp_f32 v122, v122\n"
      "v_exp_f32 v123, v123\n"
      "v_smfmac_f32_32x32x32_bf16 v[40:55], v[136:139], v[112:119], %[ix]\n"
      "ds_read_b128 v[136:139], v61 offset:1536\n"
      "global_load_dwordx4 v[174:177], %[l16], s[46:47] offset:1024 sc1\n"
      "v_exp_f32 v124, v124\n"
      "v_exp_f32 v125, v125\n"
      "v_exp_f32 v126, v126\n"
      "v_exp_f32 v127, v127\n"
      "v_exp_f32 v64, v64\n"
      "v_exp_f32 v65, v65\n"
      "s_waitcnt lgkmcnt(0)\n"
      "v_smfmac_f32_32x32x32_bf16 v[40:55], v[140:143], v[120:127], %[ix]\n"
      "ds_read_b128 v[140:143], v61 offset:2304\n"
      "v_exp_f32 v66, v66\n"
      "v_exp_f32 v67, v67\n"
      "v_exp_f32 v68, v68\n"
      "v_mfma_f32_32x32x4_2b_bf16 v[96:127], v[152:153], v[38:39], 0\n"
      "ds_read_b64 v[152:153], v60 offset:11264\n"
      "v_exp_f32 v69, v69\n"
      "v_exp_f32 v70, v70\n"
      "v_exp_f32 v71, v71\n"
      "v_exp_f32 v72, v72\n"
      "v_exp_f32 v73, v73\n"
      "v_exp_f32 v74, v74\n"
      "v_exp_f32 v75, v75\n"
      "v_exp_f32 v76, v76\n"
      "v_smfmac_f32_32x32x32_bf16 v[40:55], v[128:131], v[64:71], %[ix]\n"
      "ds_read_b128 v[128:131], v61 offset:5632\n"
      "global_load_dwordx4 v[178:181], %[l16], s[46:47] offset:2048 sc1\n"
      "v_exp_f32 v77, v77\n"
      "v_exp_f32 v78, v78\n"
      "v_exp_f32 v79, v79\n"
      "v_exp_f32 v80, v80\n"
      "v_exp_f32 v81, v81\n"
      "v_smfmac_f32_32x32x32_bf16 v[40:55], v[132:135], v[72:79], %[ix]\n"
      "ds_read_b128 v[132:135], v61 offset:6400\n"
      "global_load_dwordx4 v[182:185], %[l16], s[46:47] offset:3072 sc1\n"
      "v_exp_f32 v82, v82\n"
      "v_exp_f32 v83, v83\n"
      "v_exp_f32 v84, v84\n"
      "v_mfma_f32_32x32x16_bf16 v[96:111], v[144:147], v[34:37], v[96:111]\n"
      "ds_read_b128 v[144:147], %[l16] offset:11264\n"
      "v_exp_f32 v85, v85\n"
      "v_exp_f32 v86, v86\n"
      "v_exp_f32 v87, v87\n"
      "v_exp_f32 v88, v88\n"
      "v_mfma_f32_32x32x16_bf16 v[112:127], v[148:151], v[34:37], v[112:127]\n"
      "ds_read_b128 v[148:151], %[l16] offset:12288\n"
      "v_exp_f32 v89, v89\n"
      "v_exp_f32 v90, v90\n"
      "v_exp_f32 v91, v91\n"
      "v_smfmac_f32_32x32x32_bf16 v[40:55], v[136:139], v[80:87], %[ix]\n"
      "ds_read_b128 v[136:139], v61 offset:7168\n"
      "s_add_u32 s46, s46, 0x1000\n"
      "s_addc_u32 s47, s47, 0\n"
      "global_load_dwordx4 v[186:189], %[l16], s[46:47] offset:0 sc1\n"
      "v_exp_f32 v92, v92\n"
      "v_exp_f32 v93, v93\n"
      "v_exp_f32 v94, v94\n"
      "v_exp_f32 v95, v95\n"
      "v_exp_f32 v96, v96\n"
      "v_exp_f32 v97, v97\n"
      "s_waitcnt lgkmcnt(0)\n"
      "v_smfmac_f32_32x32x32_bf16 v[40:55], v[140:143], v[88:95], %[ix]\n"
      "ds_read_b128 v[140:143], v61 offset:7936\n"
      "v_exp_f32 v98, v98\n"
      "v_exp_f32 v99, v99\n"
      "v_exp_f32 v100, v100\n"
      "v_mfma_f32_32x32x4_2b_bf16 v[64:95], v[152:153], v[38:39], 0\n"
      "ds_read_b64 v[152:153], v60 offset:16896\n"
      "v_exp_f32 v101, v101\n"
      "v_exp_f32 v102, v102\n"
      "v_exp_f32 v103, v103\n"
      "v_exp_f32 v104, v104\n"
      "v_exp_f32 v105, v105\n"
      "v_exp_f32 v106, v106\n"
      "v_exp_f32 v107, v107\n"
      "v_exp_f32 v108, v108\n"
      "v_smfmac_f32_32x32x32_bf16 v[40:55], v[128:131], v[96:103], %[ix]\n"
      "ds_read_b128 v[128:131], v61 offset:11264\n"
      "global_load_dwordx4 v[190:193], %[l16], s[46:47] offset:1024 sc1\n"
      "v_exp_f32 v109, v109\n"
      "v_exp_f32 v110, v110\n"
      "v_exp_f32 v111, v111\n"
      "v_exp_f32 v112, v112\n"
      "v_exp_f32 v113, v113\n"
      "v_smfmac_f32_32x32x32_bf16 v[40:55], v[132:135], v[104:111], %[ix]\n"
      "ds_read_b128 v[132:135], v61 offset:12032\n"
      "global_load_dwordx4 v[194:197], %[l16], s[46:47] offset:2048 sc1\n"
      "v_exp_f32 v114, v114\n"
      "v_exp_f32 v115, v115\n"
      "v_exp_f32 v116, v116\n"
      "v_mfma_f32_32x32x16_bf16 v[64:79], v[144:147], v[34:37], v[64:79]\n"
      "ds_read_b128 v[144:147], %[l16] offset:16896\n"
      "v_exp_f32 v117, v117\n"
      "v_exp_f32 v118, v118\n"
      "v_exp_f32 v119, v119\n"
      "v_exp_f32 v120, v120\n"
      "v_mfma_f32_32x32x16_bf16 v[80:95], v[148:151], v[34:37], v[80:95]\n"
      "ds_read_b128 v[148:151], %[l16] offset:17920\n"
      "v_exp_f32 v121, v121\n"
      "v_exp_f32 v122, v122\n"
      "v_exp_f32 v123, v123\n"
      "v_smfmac_f32_32x32x32_bf16 v[40:55], v[136:139], v[112:119], %[ix]\n"
      "ds_read_b128 v[136:139], v61 offset:12800\n"
      "global_load_dwordx4 v[198:201], %[l16], s[46:47] offset:3072 sc1\n"
      "v_exp_f32 v124, v124\n"
      "v_exp_f32 v125, v125\n"
      "v_exp_f32 v126, v126\n"
      "v_exp_f32 v127, v127\n"
      "v_exp_f32 v64, v64\n"
      "v_exp_f32 v65, v65\n"
      "s_waitcnt lgkmcnt(0)\n"
      "v_smfmac_f32_32x32x32_bf16 v[40:55], v[140:143], v[120:127], %[ix]\n"
      "ds_read_b128 v[140:143], v61 offset:13568\n"
      "v_exp_f32 v66, v66\n"
      "v_exp_f32 v67, v67\n"
      "v_exp_f32 v68, v68\n"
      "v_mfma_f32_32x32x4_2b_bf16 v[96:127], v[152:153], v[38:39], 0\n"
      "ds_read_b64 v[152:153], v60 offset:22528\n"
      "v_exp_f32 v69, v69\n"
      "v_exp_f32 v70, v70\n"
      "v_exp_f32 v71, v71\n"
      "v_exp_f32 v72, v72\n"
      "v_exp_f32 v73, v73\n"
      "v_exp_f32 v74, v74\n"
      "v_exp_f32 v75, v75\n"
      "v_exp_f32 v76, v76\n"
      "v_smfmac_f32_32x32x32_bf16 v[40:55], v[128:131], v[64:71], %[ix]\n"
      "ds_read_b128 v[128:131], v61 offset:16896\n"
      "s_add_u32 s46, s46, 0x1000\n"
      "s_addc_u32 s47, s47, 0\n"
      "global_load_dwordx4 v[202:205], %[l16], s[46:47] offset:0 sc1\n"
      "v_exp_f32 v77, v77\n"
      "v_exp_f32 v78, v78\n"
      "v_exp_f32 v79, v79\n"
      "v_exp_f32 v80, v80\n"
      "v_exp_f32 v81, v81\n"
      "v_smfmac_f32_32x32x32_bf16 v[40:55], v[132:135], v[72:79], %[ix]\n"
      "ds_read_b128 v[132:135], v61 offset:17664\n"
      "global_load_dwordx4 v[206:209], %[l16], s[46:47] offset:1024 sc1\n"
      "v_exp_f32 v82, v82\n"
      "v_exp_f32 v83, v83\n"
      "v_exp_f32 v84, v84\n"
      "v_mfma_f32_32x32x16_bf16 v[96:111], v[144:147], v[34:37], v[96:111]\n"
      "ds_read_b128 v[144:147], %[l16] offset:22528\n"
      "v_exp_f32 v85, v85\n"
      "v_exp_f32 v86, v86\n"
      "v_exp_f32 v87, v87\n"
      "v_exp_f32 v88, v88\n"
      "v_mfma_f32_32x32x16_bf16 v[112:127], v[148:151], v[34:37], v[112:127]\n"
      "ds_read_b128 v[148:151], %[l16] offset:23552\n"
      "v_exp_f32 v89, v89\n"
      "v_exp_f32 v90, v90\n"
      "v_exp_f32 v91, v91\n"
      "v_smfmac_f32_32x32x32_bf16 v[40:55], v[136:139], v[80:87], %[ix]\n"
      "ds_read_b128 v[136:139], v61 offset:18432\n"
      "global_load_dwordx4 v[210:213], %[l16], s[46:47] offset:2048 sc1\n"
      "v_exp_f32 v92, v92\n"
      "v_exp_f32 v93, v93\n"
      "v_exp_f32 v94, v94\n"
      "v_exp_f32 v95, v95\n"
      "v_exp_f32 v96, v96\n"
      "v_exp_f32 v97, v97\n"
      "s_waitcnt lgkmcnt(0)\n"
      "v_smfmac_f32_32x32x32_bf16 v[40:55], v[140:143], v[88:95], %[ix]\n"
      "ds_read_b128 v[140:143], v61 offset:19200\n"
      "v_exp_f32 v98, v98\n"
      "v_exp_f32 v99, v99\n"
      "v_exp_f32 v100, v100\n"
      "v_mfma_f32_32x32x4_2b_bf16 v[64:95], v[152:153], v[38:39], 0\n"
      "ds_read_b64 v[152:153], v60 offset:28160\n"
      "v_exp_f32 v101, v101\n"
      "v_exp_f32 v102, v102\n"
      "v_exp_f32 v103, v103\n"
      "v_exp_f32 v104, v104\n"
      "v_exp_f32 v105, v105\n"
      "v_exp_f32 v106, v106\n"
      "v_exp_f32 v107, v107\n"
      "v_exp_f32 v108, v108\n"
      "v_smfmac_f32_32x32x32_bf16 v[40:55], v[128:131], v[96:103], %[ix]\n"
      "ds_read_b128 v[128:131], v61 offset:22528\n"
      "global_load_dwordx4 v[214:217], %[l16], s[46:47] offset:3072 sc1\n"
      "v_exp_f32 v109, v109\n"
      "v_exp_f32 v110, v110\n"
      "v_exp_f32 v111, v111\n"
      "v_exp_f32 v112, v112\n"
      "v_exp_f32 v113, v113\n"
      "v_smfmac_f32_32x32x32_bf16 v[40:55], v[132:135], v[104:111], %[ix]\n"
      "ds_read_b128 v[132:135], v61 offset:23296\n"
      "s_add_u32 s46, s46, 0x1000\n"
      "s_addc_u32 s47, s47, 0\n"
      "global_load_dwordx4 v[218:221], %[l16], s[46:47] offset:0 sc1\n"
      "v_exp_f32 v114, v114\n"
      "v_exp_f32 v115, v115\n"
      "v_exp_f32 v116, v116\n"
      "v_mfma_f32_32x32x16_bf16 v[64:79], v[144:147], v[34:37], v[64:79]\n"
      "ds_read_b128 v[144:147], %[l16] offset:28160\n"
      "v_exp_f32 v117, v117\n"
      "v_exp_f32 v118, v118\n"
      "v_exp_f32 v119, v119\n"
      "v_exp_f32 v120, v120\n"
      "v_mfma_f32_32x32x16_bf16 v[80:95], v[148:151], v[34:37], v[80:95]\n"
      "ds_read_b128 v[148:151], %[l16] offset:29184\n"
      "v_exp_f32 v121, v121\n"
      "v_exp_f32 v122, v122\n"
      "v_exp_f32 v123, v123\n"
      "v_smfmac_f32_32x32x32_bf16 v[40:55], v[136:139], v[112:119], %[ix]\n"
      "ds_read_b128 v[136:139], v61 offset:24064\n"
      "global_load_dwordx4 v[222:225], %[l16], s[46:47] offset:1024 sc1\n"
      "v_exp_f32 v124, v124\n"
      "v_exp_f32 v125, v125\n"
      "v_exp_f32 v126, v126\n"
      "v_exp_f32 v127, v127\n"
      "v_exp_f32 v64, v64\n"
      "v_exp_f32 v65, v65\n"
      "s_waitcnt lgkmcnt(0)\n"
      "v_smfmac_f32_32x32x32_bf16 v[40:55], v[140:143], v[120:127], %[ix]\n"
      "ds_read_b128 v[140:143], v61 offset:24832\n"
      "v_exp_f32 v66, v66\n"
      "v_exp_f32 v67, v67\n"
      "v_exp_f32 v68, v68\n"
      "v_mfma_f32_32x32x4_2b_bf16 v[96:127], v[152:153], v[38:39], 0\n"
      "ds_read_b64 v[152:153], v60 offset:33792\n"
      "v_exp_f32 v69, v69\n"
      "v_exp_f32 v70, v70\n"
      "v_exp_f32 v71, v71\n"
      "v_exp_f32 v72, v72\n"
      "v_exp_f32 v73, v73\n"
      "v_exp_f32 v74, v74\n"
      "v_exp_f32 v75, v75\n"
      "v_exp_f32 v76, v76\n"
      "v_smfmac_f32_32x32x32_bf16 v[40:55], v[128:131], v[64:71], %[ix]\n"
      "ds_read_b128 v[128:131], v61 offset:28160\n"
      "global_load_dwordx4 v[226:229], %[l16], s[46:47] offset:2048 sc1\n"
      "v_exp_f32 v77, v77\n"
      "v_exp_f32 v78, v78\n"
      "v_exp_f32 v79, v79\n"
      "v_exp_f32 v80, v80\n"
      "v_exp_f32 v81, v81\n"
      "v_smfmac_f32_32x32x32_bf16 v[40:55], v[132:135], v[72:79], %[ix]\n"
      "ds_read_b128 v[132:135], v61 offset:28928\n"
      "global_load_dwordx4 v[230:233], %[l16], s[46:47] offset:3072 sc1\n"
      "v_exp_f32 v82, v82\n"
      "v_exp_f32 v83, v83\n"
      "v_exp_f32 v84, v84\n"
      "v_mfma_f32_32x32x16_bf16 v[96:111], v[144:147], v[34:37], v[96:111]\n"
      "ds_read_b128 v[144:147], %[l16] offset:33792\n"
      "v_exp_f32 v85, v85\n"
      "v_exp_f32 v86, v86\n"
      "v_exp_f32 v87, v87\n"
      "v_exp_f32 v88, v88\n"
      "v_mfma_f32_32x32x16_bf16 v[112:127], v[148:151], v[34:37], v[112:127]\n"
      "ds_read_b128 v[148:151], %[l16] offset:34816\n"
      "v_exp_f32 v89, v89\n"
      "v_exp_f32 v90, v90\n"
      "v_exp_f32 v91, v91\n"
      "v_smfmac_f32_32x32x32_bf16 v[40:55], v[136:139], v[80:87], %[ix]\n"
      "ds_read_b128 v[136:139], v61 offset:29696\n"
      "s_add_u32 s46, s46, 0x1000\n"
      "s_addc_u32 s47, s47, 0\n"
      "global_load_dwordx4 v[234:237], %[l16], s[46:47] offset:0 sc1\n"
      "v_exp_f32 v92, v92\n"
      "v_exp_f32 v93, v93\n"
      "v_exp_f32 v94, v94\n"
      "v_exp_f32 v95, v95\n"
      "v_exp_f32 v96, v96\n"
      "v_exp_f32 v97, v97\n"
      "s_waitcnt lgkmcnt(0)\n"
      "v_smfmac_f32_32x32x32_bf16 v[40:55], v[140:143], v[88:95], %[ix]\n"
      "ds_read_b128 v[140:143], v61 offset:30464\n"
      "v_exp_f32 v98, v98\n"
      "v_exp_f32 v99, v99\n"
      "v_exp_f32 v100, v100\n"
      "v_mfma_f32_32x32x4_2b_bf16 v[64:95], v[152:153], v[38:39], 0\n"
      "ds_read_b64 v[152:153], v60 offset:39424\n"
      "v_exp_f32 v101, v101\n"
      "v_exp_f32 v102, v102\n"
      "v_exp_f32 v103, v103\n"
      "v_exp_f32 v104, v104\n"
      "v_exp_f32 v105, v105\n"
      "v_exp_f32 v106, v106\n"
      "v_exp_f32 v107, v107\n"
      "v_exp_f32 v108, v108\n"
      "v_smfmac_f32_32x32x32_bf16 v[40:55], v[128:131], v[96:103], %[ix]\n"
      "ds_read_b128 v[128:131], v61 offset:33792\n"
      "global_load_dwordx4 v[238:241], %[l16], s[46:47] offset:1024 sc1\n"
      "v_exp_f32 v109, v109\n"
      "v_exp_f32 v110, v110\n"
      "v_exp_f32 v111, v111\n"
      "v_exp_f32 v112, v112\n"
      "v_exp_f32 v113, v113\n"
      "v_smfmac_f32_32x32x32_bf16 v[40:55], v[132:135], v[104:111], %[ix]\n"
      "ds_read_b128 v[132:135], v61 offset:34560\n"
      "global_load_dwordx4 v[242:245], %[l16], s[46:47] offset:2048 sc1\n"
      "v_exp_f32 v114, v114\n"
      "v_exp_f32 v115, v115\n"
      "v_exp_f32 v116, v116\n"
      "v_mfma_f32_32x32x16_bf16 v[64:79], v[144:147], v[34:37], v[64:79]\n"
      "ds_read_b128 v[144:147], %[l16] offset:39424\n"
      "v_exp_f32 v117, v117\n"
      "v_exp_f32 v118, v118\n"
      "v_exp_f32 v119, v119\n"
      "v_exp_f32 v120, v120\n"
      "v_mfma_f32_32x32x16_bf16 v[80:95], v[148:151], v[34:37], v[80:95]\n"
      "ds_read_b128 v[148:151], %[l16] offset:40448\n"
      "v_exp_f32 v121, v121\n"
      "v_exp_f32 v122, v122\n"
      "v_exp_f32 v123, v123\n"
      "v_smfmac_f32_32x32x32_bf16 v[40:55], v[136:139], v[112:119], %[ix]\n"
      "ds_read_b128 v[136:139], v61 offset:35328\n"
      "global_load_dwordx4 v[246:249], %[l16], s[46:47] offset:3072 sc1\n"
      "v_exp_f32 v124, v124\n"
      "v_exp_f32 v125, v125\n"
      "v_exp_f32 v126, v126\n"
      "v_exp_f32 v127, v127\n"
      "v_exp_f32 v64, v64\n"
      "v_exp_f32 v65, v65\n"
      "s_waitcnt lgkmcnt(0)\n"
      "v_smfmac_f32_32x32x32_bf16 v[40:55], v[140:143], v[120:127], %[ix]\n"
      "ds_read_b128 v[140:143], v61 offset:36096\n"
      "v_exp_f32 v66, v66\n"
      "v_exp_f32 v67, v67\n"
      "v_exp_f32 v68, v68\n"
      "v_mfma_f32_32x32x4_2b_bf16 v[96:127], v[152:153], v[38:39], 0\n"
      "v_exp_f32 v69, v69\n"
      "v_exp_f32 v70, v70\n"
      "v_exp_f32 v71, v71\n"
      "v_exp_f32 v72, v72\n"
      "v_exp_f32 v73, v73\n"
      "v_exp_f32 v74, v74\n"
      "v_exp_f32 v75, v75\n"
      "v_exp_f32 v76, v76\n"
      "v_smfmac_f32_32x32x32_bf16 v[40:55], v[128:131], v[64:71], %[ix]\n"
      "ds_read_b128 v[128:131], v61 offset:39424\n"
      "v_exp_f32 v77, v77\n"
      "v_exp_f32 v78, v78\n"
      "v_exp_f32 v79, v79\n"
      "v_exp_f32 v80, v80\n"
      "v_exp_f32 v81, v81\n"
      "v_smfmac_f32_32x32x32_bf16 v[40:55], v[132:135], v[72:79], %[ix]\n"
      "ds_read_b128 v[132:135], v61 offset:40192\n"
      "v_exp_f32 v82, v82\n"
      "v_exp_f32 v83, v83\n"
      "v_exp_f32 v84, v84\n"
      "v_mfma_f32_32x32x16_bf16 v[96:111], v[144:147], v[34:37], v[96:111]\n"
      "v_exp_f32 v85, v85\n"
      "v_exp_f32 v86, v86\n"
      "v_exp_f32 v87, v87\n"
      "v_exp_f32 v88, v88\n"
      "v_mfma_f32_32x32x16_bf16 v[112:127], v[148:151], v[34:37], v[112:127]\n"
      "v_exp_f32 v89, v89\n"
      "v_exp_f32 v90, v90\n"
      "v_exp_f32 v91, v91\n"
      "v_smfmac_f32_32x32x32_bf16 v[40:55], v[136:139], v[80:87], %[ix]\n"
      "ds_read_b128 v[136:139], v61 offset:40960\n"
      "s_add_u32 s46, s46, 0x1000\n"
      "s_addc_u32 s47, s47, 0\n"
      "global_load_dwordx4 v[250:253], %[l16], s[46:47] offset:0 sc1\n"
      "v_exp_f32 v92, v92\n"
      "v_exp_f32 v93, v93\n"
      "v_exp_f32 v94, v94\n"
      "v_exp_f32 v95, v95\n"
      "v_exp_f32 v96, v96\n"
      "v_exp_f32 v97, v97\n"
      "s_waitcnt lgkmcnt(0)\n"
      "v_smfmac_f32_32x32x32_bf16 v[40:55], v[140:143], v[88:95], %[ix]\n"
      "ds_read_b128 v[140:143], v61 offset:41728\n"
      "v_exp_f32 v98, v98\n"
      "v_exp_f32 v99, v99\n"
      "v_exp_f32 v100, v100\n"
      "v_exp_f32 v101, v101\n"
      "v_exp_f32 v102, v102\n"
      "v_exp_f32 v103, v103\n"
      "v_exp_f32 v104, v104\n"
      "v_exp_f32 v105, v105\n"
      "v_exp_f32 v106, v106\n"
      "v_exp_f32 v107, v107\n"
      "v_exp_f32 v108, v108\n"
      "v_smfmac_f32_32x32x32_bf16 v[40:55], v[128:131], v[96:103], %[ix]\n"
      "v_exp_f32 v109, v109\n"
      "v_exp_f32 v110, v110\n"
      "v_exp_f32 v111, v111\n"
      "v_exp_f32 v112, v112\n"
      "v_exp_f32 v113, v113\n"
      "v_smfmac_f32_32x32x32_bf16 v[40:55], v[132:135], v[104:111], %[ix]\n"
      "v_exp_f32 v114, v114\n"
      "v_exp_f32 v115, v115\n"
      "v_exp_f32 v116, v116\n"
      "v_exp_f32 v117, v117\n"
      "v_exp_f32 v118, v118\n"
      "v_exp_f32 v119, v119\n"
      "v_exp_f32 v120, v120\n"
      "v_exp_f32 v121, v121\n"
      "v_exp_f32 v122, v122\n"
      "v_exp_f32 v123, v123\n"
      "v_smfmac_f32_32x32x32_bf16 v[40:55], v[136:139], v[112:119], %[ix]\n"
      "v_exp_f32 v124, v124\n"
      "v_exp_f32 v125, v125\n"
      "v_exp_f32 v126, v126\n"
      "v_exp_f32 v127, v127\n"
      "s_waitcnt lgkmcnt(0)\n"
      "s_nop 0\n"
      "v_smfmac_f32_32x32x32_bf16 v[40:55], v[140:143], v[120:127], %[ix]\n"
      "s_setprio 0\n"
      "s_nop 15\n"
      "v_mov_b32 v64, v48\n"
      "v_mov_b32 v65, v48\n"
      "s_nop 1\n"
      "v_permlane32_swap_b32 v64, v65\n"
      "v_add_f32 v66, |v40|, |v41|\n"
      "v_add_f32 v66, v66, |v42|\n"
      "v_add_f32 v66, v66, |v43|\n"
      "v_add_f32 v66, v66, |v44|\n"
      "v_add_f32 v66, v66, |v45|\n"
      "v_add_f32 v66, v66, |v46|\n"
      "v_add_f32 v66, v66, |v47|\n"
      "v_add_f32 v66, v66, |v48|\n"
      "v_add_f32 v66, v66, |v49|\n"
      "v_add_f32 v66, v66, |v50|\n"
      "v_add_f32 v66, v66, |v51|\n"
      "v_cmp_le_f32 vcc, 0x21800000, v65\n"
      "v_mov_b32 v67, 0x5d800000\n"
      "v_cmp_ge_f32 s[44:45], v67, v65\n"
      "v_mov_b32 v67, 0x7f800000\n"
      "s_and_b64 s[44:45], s[44:45], vcc\n"
      "v_cmp_gt_f32 vcc, v67, v66\n"
      "s_nop 1\n"
      "s_and_b64 vcc, vcc, s[44:45]\n"
      "s_cmp_eq_u64 vcc, -1\n"
      "s_cbranch_scc1 6f\n"
      "buffer_inv sc1\n"
      "s_mov_b64 s[46:47], %[kv]\n"
      "s_mov_b32 s44, 0\n"
      "v_mov_b32 v96, 0xff800000\n"
      "v_mov_b64 v[40:41], 0\n"
      "v_mov_b64 v[42:43], 0\n"
      "v_mov_b64 v[44:45], 0\n"
      "v_mov_b64 v[46:47], 0\n"
      "v_mov_b64 v[48:49], 0\n"
      "v_mov_b64 v[50:51], 0\n"
      "v_mov_b64 v[52:53], 0\n"
      "v_mov_b64 v[54:55], 0\n"
      "5:\n"
      "global_load_dwordx2 v[152:153], v60, s[46:47]\n"
      "global_load_dwordx4 v[144:147], %[l16], s[46:47]\n"
      "global_load_dwordx4 v[148:151], %[l16], s[46:47] offset:1024\n"
      "global_load_dwordx4 v[128:131], v61, s[46:47] offset:0\n"
      "global_load_dwordx4 v[132:135], v61, s[46:47] offset:768\n"
      "global_load_dwordx4 v[136:139], v61, s[46:47] offset:1536\n"
      "global_load_dwordx4 v[140:143], v61, s[46:47] offset:2304\n"
      "s_waitcnt vmcnt(0)\n"
      "v_mfma_f32_32x32x4_2b_bf16 v[64:95], v[152:153], v[38:39], 0\n"
      "s_nop 15\n"
      "s_nop 1\n"
      "v_mfma_f32_32x32x16_bf16 v[64:79], v[144:147], v[34:37], v[64:79]\n"
      "v_mfma_f32_32x32x16_bf16 v[80:95], v[148:151], v[34:37], v[80:95]\n"
      "s_nop 15\n"
      "v_max3_f32 v97, v64, v65, v96\n"
      "v_max3_f32 v97, v66, v67, v97\n"
      "v_max3_f32 v97, v68, v69, v97\n"
      "v_max3_f32 v97, v70, v71, v97\n"
      "v_max3_f32 v97, v72, v73, v97\n"
      "v_max3_f32 v97, v74, v75, v97\n"
      "v_max3_f32 v97, v76, v77, v97\n"
      "v_max3_f32 v97, v78, v79, v97\n"
      "v_max3_f32 v97, v80, v81, v97\n"
      "v_max3_f32 v97, v82, v83, v97\n"
      "v_max3_f32 v97, v84, v85, v97\n"
      "v_max3_f32 v97, v86, v87, v97\n"
      "v_max3_f32 v97, v88, v89, v97\n"
      "v_max3_f32 v97, v90, v91, v97\n"
      "v_max3_f32 v97, v92, v93, v97\n"
      "v_max3_f32 v97, v94, v95, v97\n"
      "v_mov_b32 v98, v97\n"
      "s_nop 1\n"
      "v_permlane32_swap_b32 v97, v98\n"
      "v_max_f32 v97, v97, v98\n"
      "v_cmp_neq_f32 vcc, 0xff800000, v97\n"
      "s_nop 1\n"
      "v_cndmask_b32 v98, 0, v97, vcc\n"
      "v_sub_f32 v99, v96, v98\n"
      "v_exp_f32 v99, v99\n"
      "v_mov_b32 v96, v97\n"
      "v_sub_f32 v64, v64, v98\n"
      "v_sub_f32 v65, v65, v98\n"
      "v_sub_f32 v66, v66, v98\n"
      "v_sub_f32 v67, v67, v98\n"
      "v_sub_f32 v68, v68, v98\n"
      "v_sub_f32 v69, v69, v98\n"
      "v_sub_f32 v70, v70, v98\n"
      "v_sub_f32 v71, v71, v98\n"
      "v_sub_f32 v72, v72, v98\n"
      "v_sub_f32 v73, v73, v98\n"
      "v_sub_f32 v74, v74, v98\n"
      "v_sub_f32 v75, v75, v98\n"
      "v_sub_f32 v76, v76, v98\n"
      "v_sub_f32 v77, v77, v98\n"
      "v_sub_f32 v78, v78, v98\n"
      "v_sub_f32 v79, v79, v98\n"
      "v_sub_f32 v80, v80, v98\n"
      "v_sub_f32 v81, v81, v98\n"
      "v_sub_f32 v82, v82, v98\n"
      "v_sub_f32 v83, v83, v98\n"
      "v_sub_f32 v84, v84, v98\n"
      "v_sub_f32 v85, v85, v98\n"
      "v_sub_f32 v86, v86, v98\n"
      "v_sub_f32 v87, v87, v98\n"
      "v_sub_f32 v88, v88, v98\n"
      "v_sub_f32 v89, v89, v98\n"
      "v_sub_f32 v90, v90, v98\n"
      "v_sub_f32 v91, v91, v98\n"
      "v_sub_f32 v92, v92, v98\n"
      "v_sub_f32 v93, v93, v98\n"
      "v_sub_f32 v94, v94, v98\n"
      "v_sub_f32 v95, v95, v98\n"
      "v_exp_f32 v64, v64\n"
      "v_exp_f32 v65, v65\n"
      "v_exp_f32 v66, v66\n"
      "v_exp_f32 v67, v67\n"
      "v_exp_f32 v68, v68\n"
      "v_exp_f32 v69, v69\n"
      "v_exp_f32 v70, v70\n"
      "v_exp_f32 v71, v71\n"
      "v_exp_f32 v72, v72\n"
      "v_exp_f32 v73, v73\n"
      "v_exp_f32 v74, v74\n"
      "v_exp_f32 v75, v75\n"
      "v_exp_f32 v76, v76\n"
      "v_exp_f32 v77, v77\n"
      "v_exp_f32 v78, v78\n"
      "v_exp_f32 v79, v79\n"
      "v_exp_f32 v80, v80\n"
      "v_exp_f32 v81, v81\n"
      "v_exp_f32 v82, v82\n"
      "v_exp_f32 v83, v83\n"
      "v_exp_f32 v84, v84\n"
      "v_exp_f32 v85, v85\n"
      "v_exp_f32 v86, v86\n"
      "v_exp_f32 v87, v87\n"
      "v_exp_f32 v88, v88\n"
      "v_exp_f32 v89, v89\n"
      "v_exp_f32 v90, v90\n"
      "v_exp_f32 v91, v91\n"
      "v_exp_f32 v92, v92\n"
      "v_exp_f32 v93, v93\n"
      "v_exp_f32 v94, v94\n"
      "v_exp_f32 v95, v95\n"
      "v_mul_f32 v40, v40, v99\n"
      "v_mul_f32 v41, v41, v99\n"
      "v_mul_f32 v42, v42, v99\n"
      "v_mul_f32 v43, v43, v99\n"
      "v_mul_f32 v44, v44, v99\n"
      "v_mul_f32 v45, v45, v99\n"
      "v_mul_f32 v46, v46, v99\n"
      "v_mul_f32 v47, v47, v99\n"
      "v_mul_f32 v48, v48, v99\n"
      "v_mul_f32 v49, v49, v99\n"
      "v_mul_f32 v50, v50, v99\n"
      "v_mul_f32 v51, v51, v99\n"
      "v_mul_f32 v52, v52, v99\n"
      "v_mul_f32 v53, v53, v99\n"
      "v_mul_f32 v54, v54, v99\n"
      "v_mul_f32 v55, v55, v99\n"
      "s_nop 1\n"
      "v_smfmac_f32_32x32x32_bf16 v[40:55], v[128:131], v[64:71], %[ix]\n"
      "v_smfmac_f32_32x32x32_bf16 v[40:55], v[132:135], v[72:79], %[ix]\n"
      "v_smfmac_f32_32x32x32_bf16 v[40:55], v[136:139], v[80:87], %[ix]\n"
      "v_smfmac_f32_32x32x32_bf16 v[40:55], v[140:143], v[88:95], %[ix]\n"
      "s_add_u32 s46, s46, 5632\n"
      "s_addc_u32 s47, s47, 0\n"
      "s_add_u32 s44, s44, 1\n"
      "s_cmp_lt_u32 s44, 32\n"
      "s_nop 15\n"
      "s_cbranch_scc1 5b\n"
      "v_mov_b32 v64, v48\n"
      "v_mov_b32 v65, v48\n"
      "s_nop 1\n"
      "v_permlane32_swap_b32 v64, v65\n"
      "6:\n"
      "v_rcp_f32 v65, v65\n"
      "v_add_u32 v72, %[ost], %[l16]\n"
      "v_add_u32 v73, %[ostb], %[r8]\n"
      "v_mul_f32 v40, v40, v65\n"
      "v_mul_f32 v41, v41, v65\n"
      "v_mul_f32 v42, v42, v65\n"
      "v_mul_f32 v43, v43, v65\n"
      "v_mul_f32 v44, v44, v65\n"
      "v_mul_f32 v45, v45, v65\n"
      "v_mul_f32 v46, v46, v65\n"
      "v_mul_f32 v47, v47, v65\n"
      "v_mul_f32 v48, v48, v65\n"
      "v_mul_f32 v49, v49, v65\n"
      "v_mul_f32 v50, v50, v65\n"
      "v_mul_f32 v51, v51, v65\n"
      "v_cvt_pk_bf16_f32 v66, v40, v41\n"
      "v_cvt_pk_bf16_f32 v67, v42, v43\n"
      "v_cvt_pk_bf16_f32 v68, v44, v45\n"
      "v_cvt_pk_bf16_f32 v69, v46, v47\n"
      "v_cvt_pk_bf16_f32 v70, v48, v49\n"
      "v_cvt_pk_bf16_f32 v71, v50, v51\n"
      "global_store_dwordx4 v72, v[66:69], %[ws] sc1\n"
      "s_mov_b32 exec_hi, 0\n"
      "global_store_dwordx2 v73, v[70:71], %[ws] sc1\n"
      "s_mov_b32 exec_hi, -1\n"
      "s_nop 0\n"
      : [g] "+&v"(g), [av] "+&{v24}"(av), WR("+&", "=&")
      : [l16] "v"(l16), [qm] "s"(L_QA + w * 1024), [qn] "s"(L_QB + w * 256), [ix] "v"(ix), [m0b] "s"(m0b), [m0c] "s"(m0c), [m0d] "s"(m0d), [kv] "s"(kv),
        [w] "s"(w), [tag] "v"(tag), [f3] "s"(f3), [ws] "s"(ws), [wf] "s"(wf), [ost] "s"(ost), [ostb] "s"(ostb), [r8] "v"(r8),
        [grp] "s"(gy), [ys] "{v25}"(ys), [tw] "{v26}"(tw), [tr] "{v27}"(tr), [yo] "{v22}"(yo)
      : "memory", "scc", "vcc", "s44", "s45", "s46", "s47", "v34", "v35", "v36", "v37", "v38", "v39", V10(4), "v50", "v51", "v52", "v53", "v54", "v55", "v56", "v57", "v59", V10(6), V10(7),
        V10(8), V10(9), V10(10), V10(11), V10(12), V10(13), V10(14), "v150", "v151", "v152", "v153");
  }

  {
    const unsigned og1 = 32768 + w * 1024, om0 = L_O + m0b, om1 = L_O + og1;
    const unsigned po2 = (F2L ? WS_F2 + (tt * 8 + w) * 128 : WS_F2 + (tt * 32 + 4 * w) * 8) + ((l16 >> 1) & 24);
    if (w < 4) {
      const unsigned dko = 320 * xp + (l16 < 640 ? l16 >> 1 : 0), ya0 = (32 * rb + 8 * w) * 1280 + dko, a1 = ya0 + 4 * 1280;
      const unsigned yi0 = WS_YT + dko * (YT_ROW / 2) + (32 * rb + 8 * w) * 2, f3o = WS_F3 + xp * 512 + (l16 >> 1);
      u32x2 ui[8], dk;
      u32x4 yi[4];
      asm volatile(
      "global_load_dwordx2 %[u0], %[a0], %[u] offset:0\n"
      "global_load_dwordx2 %[u1], %[a0], %[u] offset:1280\n"
      "global_load_dwordx2 %[u2], %[a0], %[u] offset:2560\n"
      "global_load_dwordx2 %[u3], %[a0], %[u] offset:3840\n"
      "global_load_dwordx2 %[u4], %[a1], %[u] offset:0\n"
      "global_load_dwordx2 %[u5], %[a1], %[u] offset:1280\n"
      "global_load_dwordx2 %[u6], %[a1], %[u] offset:2560\n"
      "global_load_dwordx2 %[u7], %[a1], %[u] offset:3840\n"
      "global_load_dwordx2 %[dk], %[dko], %[dsk]\n"
      "global_load_dwordx2 v[64:65], %[f3o], %[ws] sc1\n"
      "s_waitcnt vmcnt(10)\n"
      "s_mov_b64 exec, 1\n"
      "v_mov_b32 v66, %[f2]\n"
      "global_store_dwordx2 v66, %[tag], %[ws] sc1\n"
      "s_mov_b64 exec, -1\n"
      "s_mov_b32 s44, 0\n"
      "s_waitcnt vmcnt(1)\n"
      "5:\n"
      "v_cmp_eq_u64 vcc, v[64:65], %[tag]\n"
      "s_nop 1\n"
      "s_cmp_eq_u64 vcc, -1\n"
      "s_cbranch_scc1 6f\n"
      "s_add_u32 s44, s44, 1\n"
      "s_cmp_gt_u32 s44, 0x8000\n"
      "s_cbranch_scc1 6f\n"
      "s_sleep 1\n"
      "global_load_dwordx2 v[64:65], %[f3o], %[ws] sc1\n"
      "s_waitcnt vmcnt(0)\n"
      "s_branch 5b\n"
      "6:\n"
      "v_add_u32 v67, 8448, %[yo]\n"
      "s_add_u32 s46, %[wsl], 4224\n"
      "s_addc_u32 s47, %[wsh], 0\n"
      "global_load_dwordx4 %[y0], %[yo], %[ws] sc1\n"
      "global_load_dwordx4 %[y1], %[yo], s[46:47] sc1\n"
      "global_load_dwordx4 %[y2], v67, %[ws] sc1\n"
      "global_load_dwordx4 %[y3], v67, s[46:47] sc1\n"
      "s_waitcnt vmcnt(0)\n"
        : [u0] "=&v"(ui[0]), [u1] "=&v"(ui[1]), [u2] "=&v"(ui[2]), [u3] "=&v"(ui[3]), [u4] "=&v"(ui[4]), [u5] "=&v"(ui[5]),
          [u6] "=&v"(ui[6]), [u7] "=&v"(ui[7]), [dk] "=&v"(dk), [y0] "=&v"(yi[0]), [y1] "=&v"(yi[1]), [y2] "=&v"(yi[2]),
          [y3] "=&v"(yi[3])
        : [tag] "v"(tag), [ws] "s"(ws), [f2] "s"(f2), [a0] "v"(ya0), [a1] "v"(a1), [u] "s"(u), [dko] "v"(dko), [dsk] "s"(Dsk), [f3o] "v"(f3o), [yo] "v"(yi0),
          [wsl] "s"((unsigned)(u64)ws), [wsh] "s"((unsigned)((u64)ws >> 32))
        : "memory", "scc", "vcc", "s44", "s46", "s47", "v64", "v65", "v66", "v67");
      if (l16 < 640) {
        const f32x2 dkf[2] = {{bf_lo(dk[0]), bf_hi(dk[0])}, {bf_lo(dk[1]), bf_hi(dk[1])}};
        const unsigned oo = 2 * ya0 - dko;
#pragma unroll
        for (int i = 0; i < 8; ++i) {
          u32x2 v;
#pragma unroll
          for (int j = 0; j < 2; ++j) {
            const f32x2 p = (f32x2){bf_lo(ui[i][j]), bf_hi(ui[i][j])} * dkf[j];
            const unsigned sk = pk_bf16(p[0], p[1]), ya = yi[2 * j][i >> 1], yb = yi[2 * j + 1][i >> 1];
            const f32x2 s = (f32x2){(i & 1) ? bf_hi(ya) : bf_lo(ya), (i & 1) ? bf_hi(yb) : bf_lo(yb)} + (f32x2){bf_lo(sk), bf_hi(sk)};
            v[j] = pk_bf16(s[0], s[1]);
          }
          asm volatile("global_store_dwordx2 %0, %1, %2 offset:%3 sc1" ::"v"(oo), "v"(v), "s"(out + (i >> 1) * 2 * OUTW), "n"((i & 1) * 2560) : "memory");
        }
      }
    } else {
      asm volatile(
      "s_waitcnt vmcnt(0)\n"
      "s_mov_b64 exec, 1\n"
      "v_mov_b32 v66, %[f2]\n"
      "global_store_dwordx2 v66, %[tag], %[ws] sc1\n"
      "s_mov_b64 exec, -1\n"
        :
        : [tag] "v"(tag), [ws] "s"(ws), [f2] "s"(f2)
        : "memory", "v66");
    }
    const unsigned oa = L_O + l16, ps = L_OP + l16, ob = L_O + 32768 + (l16 & 512) + r8;
    unsigned odst = ((tt * 32 + ((l16 >> 4) & 15)) * OUTW + DM + (cg * 5 + ((5 * w + 4) >> 3)) * 32) * 2 + ((l16 >> 5) & 16) + ((l16 >> 3) & 32);
    asm volatile("" : "+v"(odst));
    f32x16 c;
    asm volatile(
      "s_barrier\n"
      "global_load_dwordx2 v[64:65], %[po], %[ws] sc1\n"
      "s_mov_b32 s44, 0\n"
      "s_waitcnt vmcnt(0)\n"
      "3:\n"
      "v_cmp_eq_u64 vcc, v[64:65], %[tag]\n"
      "s_nop 1\n"
      "s_cmp_eq_u64 vcc, -1\n"
      "s_cbranch_scc1 4f\n"
      "s_add_u32 s44, s44, 1\n"
      "s_cmp_gt_u32 s44, 0x8000\n"
      "s_cbranch_scc1 4f\n"
      "s_sleep 1\n"
      "global_load_dwordx2 v[64:65], %[po], %[ws] sc1\n"
      "s_waitcnt vmcnt(0)\n"
      "s_branch 3b\n"
      "4:\n"
      "s_mov_b32 m0, %[om0]\n"
      "v_add_u32 v70, %[m0b], %[l16]\n"
      "global_load_lds_dwordx4 v70, %[ot] offset:0 sc1\n"
      "global_load_lds_dwordx4 v70, %[ot] offset:1024 sc1\n"
      "global_load_lds_dwordx4 v70, %[ot] offset:2048 sc1\n"
      "global_load_lds_dwordx4 v70, %[ot] offset:3072 sc1\n"
      "s_mov_b32 m0, %[om1]\n"
      "v_add_u32 v70, %[og1], %[l16]\n"
      "global_load_lds_dwordx4 v70, %[ot] sc1\n"
      "s_waitcnt vmcnt(0)\n"
      "s_barrier\n"
      "s_cmp_eq_u32 %[w], 1\n"
      "s_cbranch_scc1 11f\n"
      "s_cmp_eq_u32 %[w], 2\n"
      "s_cbranch_scc1 12f\n"
      "s_cmp_eq_u32 %[w], 3\n"
      "s_cbranch_scc1 13f\n"
      "s_cmp_eq_u32 %[w], 4\n"
      "s_cbranch_scc1 14f\n"
      "s_cmp_eq_u32 %[w], 5\n"
      "s_cbranch_scc1 15f\n"
      "s_cmp_eq_u32 %[w], 6\n"
      "s_cbranch_scc1 16f\n"
      "s_cmp_eq_u32 %[w], 7\n"
      "s_cbranch_scc1 17f\n"
      "10:\n"
      "ds_read_b128 v[80:83], %[oa] offset:0\n"
      "ds_read_b128 v[84:87], %[oa] offset:1024\n"
      "ds_read_b128 v[88:91], %[oa] offset:2048\n"
      "ds_read_b128 v[92:95], %[oa] offset:3072\n"
      "s_waitcnt lgkmcnt(3)\n"
      "v_mfma_f32_32x32x16_bf16 v[40:55], v[154:157], v[80:83], 0\n"
      "ds_read_b128 v[80:83], %[oa] offset:4096\n"
      "s_waitcnt lgkmcnt(3)\n"
      "v_mfma_f32_32x32x16_bf16 v[40:55], v[158:161], v[84:87], v[40:55]\n"
      "ds_read_b128 v[84:87], %[oa] offset:5120\n"
      "s_waitcnt lgkmcnt(3)\n"
      "v_mfma_f32_32x32x16_bf16 v[40:55], v[162:165], v[88:91], v[40:55]\n"
      "ds_read_b128 v[88:91], %[oa] offset:6144\n"
      "s_waitcnt lgkmcnt(3)\n"
      "v_mfma_f32_32x32x16_bf16 v[40:55], v[166:169], v[92:95], v[40:55]\n"
      "ds_read_b128 v[92:95], %[oa] offset:7168\n"
      "s_waitcnt lgkmcnt(3)\n"
      "v_mfma_f32_32x32x16_bf16 v[40:55], v[170:173], v[80:83], v[40:55]\n"
      "ds_read_b128 v[80:83], %[oa] offset:8192\n"
      "s_waitcnt lgkmcnt(3)\n"
      "v_mfma_f32_32x32x16_bf16 v[40:55], v[174:177], v[84:87], v[40:55]\n"
      "ds_read_b128 v[84:87], %[oa] offset:9216\n"
      "s_waitcnt lgkmcnt(3)\n"
      "v_mfma_f32_32x32x16_bf16 v[40:55], v[178:181], v[88:91], v[40:55]\n"
      "ds_read_b128 v[88:91], %[oa] offset:10240\n"
      "s_waitcnt lgkmcnt(3)\n"
      "v_mfma_f32_32x32x16_bf16 v[40:55], v[182:185], v[92:95], v[40:55]\n"
      "ds_read_b128 v[92:95], %[oa] offset:11264\n"
      "s_waitcnt lgkmcnt(3)\n"
      "v_mfma_f32_32x32x16_bf16 v[40:55], v[186:189], v[80:83], v[40:55]\n"
      "ds_read_b128 v[80:83], %[oa] offset:12288\n"
      "s_waitcnt lgkmcnt(3)\n"
      "v_mfma_f32_32x32x16_bf16 v[40:55], v[190:193], v[84:87], v[40:55]\n"
      "ds_read_b128 v[84:87], %[oa] offset:13312\n"
      "s_waitcnt lgkmcnt(3)\n"
      "v_mfma_f32_32x32x16_bf16 v[40:55], v[194:197], v[88:91], v[40:55]\n"
      "ds_read_b128 v[88:91], %[oa] offset:14336\n"
      "s_waitcnt lgkmcnt(3)\n"
      "v_mfma_f32_32x32x16_bf16 v[40:55], v[198:201], v[92:95], v[40:55]\n"
      "ds_read_b128 v[92:95], %[oa] offset:15360\n"
      "s_waitcnt lgkmcnt(3)\n"
      "v_mfma_f32_32x32x16_bf16 v[40:55], v[202:205], v[80:83], v[40:55]\n"
      "ds_read_b128 v[80:83], %[oa] offset:16384\n"
      "s_waitcnt lgkmcnt(3)\n"
      "v_mfma_f32_32x32x16_bf16 v[40:55], v[206:209], v[84:87], v[40:55]\n"
      "ds_read_b128 v[84:87], %[oa] offset:17408\n"
      "s_waitcnt lgkmcnt(3)\n"
      "v_mfma_f32_32x32x16_bf16 v[40:55], v[210:213], v[88:91], v[40:55]\n"
      "ds_read_b128 v[88:91], %[oa] offset:18432\n"
      "s_waitcnt lgkmcnt(3)\n"
      "v_mfma_f32_32x32x16_bf16 v[40:55], v[214:217], v[92:95], v[40:55]\n"
      "ds_read_b128 v[92:95], %[oa] offset:19456\n"
      "s_waitcnt lgkmcnt(3)\n"
      "v_mfma_f32_32x32x16_bf16 v[40:55], v[218:221], v[80:83], v[40:55]\n"
      "ds_read_b128 v[80:83], %[oa] offset:20480\n"
      "s_waitcnt lgkmcnt(3)\n"
      "v_mfma_f32_32x32x16_bf16 v[40:55], v[222:225], v[84:87], v[40:55]\n"
      "ds_read_b128 v[84:87], %[oa] offset:21504\n"
      "s_waitcnt lgkmcnt(3)\n"
      "v_mfma_f32_32x32x16_bf16 v[40:55], v[226:229], v[88:91], v[40:55]\n"
      "ds_read_b128 v[88:91], %[oa] offset:22528\n"
      "s_waitcnt lgkmcnt(3)\n"
      "v_mfma_f32_32x32x16_bf16 v[40:55], v[230:233], v[92:95], v[40:55]\n"
      "ds_read_b128 v[92:95], %[oa] offset:23552\n"
      "s_waitcnt lgkmcnt(3)\n"
      "v_mfma_f32_32x32x16_bf16 v[40:55], v[234:237], v[80:83], v[40:55]\n"
      "ds_read_b128 v[80:83], %[oa] offset:24576\n"
      "s_waitcnt lgkmcnt(3)\n"
      "v_mfma_f32_32x32x16_bf16 v[40:55], v[238:241], v[84:87], v[40:55]\n"
      "s_waitcnt lgkmcnt(0)\n"
      "s_barrier\n"
      "ds_read_b128 v[96:99], %[ps] offset:0\n"
      "ds_read_b128 v[100:103], %[ps] offset:1024\n"
      "ds_read_b128 v[104:107], %[ps] offset:2048\n"
      "ds_read_b128 v[108:111], %[ps] offset:3072\n"
      "v_mfma_f32_32x32x16_bf16 v[40:55], v[242:245], v[88:91], v[40:55]\n"
      "v_mfma_f32_32x32x16_bf16 v[40:55], v[246:249], v[92:95], v[40:55]\n"
      "v_mfma_f32_32x32x16_bf16 v[40:55], v[250:253], v[80:83], v[40:55]\n"
      "s_nop 15\n"
      "s_waitcnt lgkmcnt(0)\n"
      "v_pk_add_f32 v[40:41], v[40:41], v[96:97]\n"
      "v_pk_add_f32 v[42:43], v[42:43], v[98:99]\n"
      "v_pk_add_f32 v[44:45], v[44:45], v[100:101]\n"
      "v_pk_add_f32 v[46:47], v[46:47], v[102:103]\n"
      "v_pk_add_f32 v[48:49], v[48:49], v[104:105]\n"
      "v_pk_add_f32 v[50:51], v[50:51], v[106:107]\n"
      "v_pk_add_f32 v[52:53], v[52:53], v[108:109]\n"
      "v_pk_add_f32 v[54:55], v[54:55], v[110:111]\n"
      "s_branch 19f\n"
      "11:\n"
      "s_setprio 1\n"
      "ds_read_b128 v[80:83], %[oa] offset:25600\n"
      "ds_read_b128 v[84:87], %[oa] offset:26624\n"
      "ds_read_b128 v[88:91], %[oa] offset:27648\n"
      "ds_read_b128 v[92:95], %[oa] offset:28672\n"
      "s_waitcnt lgkmcnt(3)\n"
      "v_mfma_f32_32x32x16_bf16 v[40:55], v[154:157], v[80:83], 0\n"
      "ds_read_b128 v[80:83], %[oa] offset:29696\n"
      "s_waitcnt lgkmcnt(3)\n"
      "v_mfma_f32_32x32x16_bf16 v[40:55], v[158:161], v[84:87], v[40:55]\n"
      "ds_read_b128 v[84:87], %[oa] offset:30720\n"
      "s_waitcnt lgkmcnt(3)\n"
      "v_mfma_f32_32x32x16_bf16 v[40:55], v[162:165], v[88:91], v[40:55]\n"
      "ds_read_b128 v[88:91], %[oa] offset:31744\n"
      "s_waitcnt lgkmcnt(3)\n"
      "v_mfma_f32_32x32x16_bf16 v[40:55], v[166:169], v[92:95], v[40:55]\n"
      "ds_read_b64 v[92:93], %[ob] offset:0\n"
      "ds_read_b64 v[94:95], %[ob] offset:256\n"
      "s_waitcnt lgkmcnt(4)\n"
      "v_mfma_f32_32x32x16_bf16 v[40:55], v[170:173], v[80:83], v[40:55]\n"
      "ds_read_b64 v[80:81], %[ob] offset:1024\n"
      "ds_read_b64 v[82:83], %[ob] offset:1280\n"
      "s_waitcnt lgkmcnt(5)\n"
      "v_mfma_f32_32x32x16_bf16 v[40:55], v[174:177], v[84:87], v[40:55]\n"
      "ds_read_b64 v[84:85], %[ob] offset:2048\n"
      "ds_read_b64 v[86:87], %[ob] offset:2304\n"
      "s_waitcnt lgkmcnt(6)\n"
      "v_mfma_f32_32x32x16_bf16 v[40:55], v[178:181], v[88:91], v[40:55]\n"
      "ds_read_b64 v[88:89], %[ob] offset:3072\n"
      "ds_read_b64 v[90:91], %[ob] offset:3328\n"
      "s_waitcnt lgkmcnt(6)\n"
      "v_mfma_f32_32x32x16_bf16 v[40:55], v[182:185], v[92:95], v[40:55]\n"
      "ds_read_b64 v[92:93], %[ob] offset:4096\n"
      "ds_read_b64 v[94:95], %[ob] offset:4352\n"
      "s_waitcnt lgkmcnt(6)\n"
      "v_mfma_f32_32x32x16_bf16 v[40:55], v[186:189], v[80:83], v[40:55]\n"
      "ds_read_b64 v[80:81], %[ob] offset:5120\n"
      "ds_read_b64 v[82:83], %[ob] offset:5376\n"
      "s_waitcnt lgkmcnt(6)\n"
      "v_mfma_f32_32x32x16_bf16 v[40:55], v[190:193], v[84:87], v[40:55]\n"
      "ds_read_b64 v[84:85], %[ob] offset:6144\n"
      "ds_read_b64 v[86:87], %[ob] offset:6400\n"
      "s_waitcnt lgkmcnt(6)\n"
      "v_mfma_f32_32x32x16_bf16 v[40:55], v[194:197], v[88:91], v[40:55]\n"
      "ds_read_b64 v[88:89], %[ob] offset:7168\n"
      "ds_read_b64 v[90:91], %[ob] offset:7424\n"
      "s_waitcnt lgkmcnt(6)\n"
      "v_mfma_f32_32x32x16_bf16 v[40:55], v[198:201], v[92:95], v[40:55]\n"
      "ds_read_b128 v[92:95], %[oa] offset:0\n"
      "s_waitcnt lgkmcnt(5)\n"
      "v_mfma_f32_32x32x16_bf16 v[40:55], v[202:205], v[80:83], v[40:55]\n"
      "ds_read_b128 v[80:83], %[oa] offset:1024\n"
      "s_waitcnt lgkmcnt(4)\n"
      "v_mfma_f32_32x32x16_bf16 v[40:55], v[206:209], v[84:87], v[40:55]\n"
      "ds_read_b128 v[84:87], %[oa] offset:2048\n"
      "s_waitcnt lgkmcnt(3)\n"
      "v_mfma_f32_32x32x16_bf16 v[40:55], v[210:213], v[88:91], v[40:55]\n"
      "ds_read_b128 v[88:91], %[oa] offset:3072\n"
      "s_waitcnt lgkmcnt(3)\n"
      "v_mfma_f32_32x32x16_bf16 v[64:79], v[214:217], v[92:95], 0\n"
      "ds_read_b128 v[92:95], %[oa] offset:4096\n"
      "s_waitcnt lgkmcnt(3)\n"
      "v_mfma_f32_32x32x16_bf16 v[64:79], v[218:221], v[80:83], v[64:79]\n"
      "ds_read_b128 v[80:83], %[oa] offset:5120\n"
      "s_waitcnt lgkmcnt(3)\n"
      "v_mfma_f32_32x32x16_bf16 v[64:79], v[222:225], v[84:87], v[64:79]\n"
      "ds_read_b128 v[84:87], %[oa] offset:6144\n"
      "s_waitcnt lgkmcnt(3)\n"
      "v_mfma_f32_32x32x16_bf16 v[64:79], v[226:229], v[88:91], v[64:79]\n"
      "ds_read_b128 v[88:91], %[oa] offset:7168\n"
      "s_nop 3\n"
      "ds_write_b128 %[ps], v[40:43] offset:0\n"
      "ds_write_b128 %[ps], v[44:47] offset:1024\n"
      "ds_write_b128 %[ps], v[48:51] offset:2048\n"
      "ds_write_b128 %[ps], v[52:55] offset:3072\n"
      "s_waitcnt lgkmcnt(7)\n"
      "v_mfma_f32_32x32x16_bf16 v[64:79], v[230:233], v[92:95], v[64:79]\n"
      "ds_read_b128 v[92:95], %[oa] offset:8192\n"
      "s_waitcnt lgkmcnt(7)\n"
      "v_mfma_f32_32x32x16_bf16 v[64:79], v[234:237], v[80:83], v[64:79]\n"
      "ds_read_b128 v[80:83], %[oa] offset:9216\n"
      "s_waitcnt lgkmcnt(7)\n"
      "v_mfma_f32_32x32x16_bf16 v[64:79], v[238:241], v[84:87], v[64:79]\n"
      "s_waitcnt lgkmcnt(6)\n"
      "v_mfma_f32_32x32x16_bf16 v[64:79], v[242:245], v[88:91], v[64:79]\n"
      "s_waitcnt lgkmcnt(1)\n"
      "v_mfma_f32_32x32x16_bf16 v[64:79], v[246:249], v[92:95], v[64:79]\n"
      "s_waitcnt lgkmcnt(0)\n"
      "v_mfma_f32_32x32x16_bf16 v[64:79], v[250:253], v[80:83], v[64:79]\n"
      "s_nop 15\n"
      "ds_write_b128 %[ps], v[64:67] offset:4096\n"
      "ds_write_b128 %[ps], v[68:71] offset:5120\n"
      "ds_write_b128 %[ps], v[72:75] offset:6144\n"
      "ds_write_b128 %[ps], v[76:79] offset:7168\n"
      "s_waitcnt lgkmcnt(0)\n"
      "s_barrier\n"
      "s_branch 19f\n"
      "12:\n"
      "ds_read_b128 v[80:83], %[oa] offset:10240\n"
      "ds_read_b128 v[84:87], %[oa] offset:11264\n"
      "ds_read_b128 v[88:91], %[oa] offset:12288\n"
      "ds_read_b128 v[92:95], %[oa] offset:13312\n"
      "s_waitcnt lgkmcnt(3)\n"
      "v_mfma_f32_32x32x16_bf16 v[40:55], v[154:157], v[80:83], 0\n"
      "ds_read_b128 v[80:83], %[oa] offset:14336\n"
      "s_waitcnt lgkmcnt(3)\n"
      "v_mfma_f32_32x32x16_bf16 v[40:55], v[158:161], v[84:87], v[40:55]\n"
      "ds_read_b128 v[84:87], %[oa] offset:15360\n"
      "s_waitcnt lgkmcnt(3)\n"
      "v_mfma_f32_32x32x16_bf16 v[40:55], v[162:165], v[88:91], v[40:55]\n"
      "ds_read_b128 v[88:91], %[oa] offset:16384\n"
      "s_waitcnt lgkmcnt(3)\n"
      "v_mfma_f32_32x32x16_bf16 v[40:55], v[166:169], v[92:95], v[40:55]\n"
      "ds_read_b128 v[92:95], %[oa] offset:17408\n"
      "s_waitcnt lgkmcnt(3)\n"
      "v_mfma_f32_32x32x16_bf16 v[40:55], v[170:173], v[80:83], v[40:55]\n"
      "ds_read_b128 v[80:83], %[oa] offset:18432\n"
      "s_waitcnt lgkmcnt(3)\n"
      "v_mfma_f32_32x32x16_bf16 v[40:55], v[174:177], v[84:87], v[40:55]\n"
      "ds_read_b128 v[84:87], %[oa] offset:19456\n"
      "s_waitcnt lgkmcnt(3)\n"
      "v_mfma_f32_32x32x16_bf16 v[40:55], v[178:181], v[88:91], v[40:55]\n"
      "ds_read_b128 v[88:91], %[oa] offset:20480\n"
      "s_waitcnt lgkmcnt(3)\n"
      "v_mfma_f32_32x32x16_bf16 v[40:55], v[182:185], v[92:95], v[40:55]\n"
      "ds_read_b128 v[92:95], %[oa] offset:21504\n"
      "s_waitcnt lgkmcnt(3)\n"
      "v_mfma_f32_32x32x16_bf16 v[40:55], v[186:189], v[80:83], v[40:55]\n"
      "ds_read_b128 v[80:83], %[oa] offset:22528\n"
      "s_waitcnt lgkmcnt(3)\n"
      "v_mfma_f32_32x32x16_bf16 v[40:55], v[190:193], v[84:87], v[40:55]\n"
      "ds_read_b128 v[84:87], %[oa] offset:23552\n"
      "s_waitcnt lgkmcnt(3)\n"
      "v_mfma_f32_32x32x16_bf16 v[40:55], v[194:197], v[88:91], v[40:55]\n"
      "ds_read_b128 v[88:91], %[oa] offset:24576\n"
      "s_waitcnt lgkmcnt(3)\n"
      "v_mfma_f32_32x32x16_bf16 v[40:55], v[198:201], v[92:95], v[40:55]\n"
      "ds_read_b128 v[92:95], %[oa] offset:25600\n"
      "s_waitcnt lgkmcnt(3)\n"
      "v_mfma_f32_32x32x16_bf16 v[40:55], v[202:205], v[80:83], v[40:55]\n"
      "ds_read_b128 v[80:83], %[oa] offset:26624\n"
      "s_waitcnt lgkmcnt(3)\n"
      "v_mfma_f32_32x32x16_bf16 v[40:55], v[206:209], v[84:87], v[40:55]\n"
      "ds_read_b128 v[84:87], %[oa] offset:27648\n"
      "s_waitcnt lgkmcnt(3)\n"
      "v_mfma_f32_32x32x16_bf16 v[40:55], v[210:213], v[88:91], v[40:55]\n"
      "ds_read_b128 v[88:91], %[oa] offset:28672\n"
      "s_waitcnt lgkmcnt(3)\n"
      "v_mfma_f32_32x32x16_bf16 v[40:55], v[214:217], v[92:95], v[40:55]\n"
      "ds_read_b128 v[92:95], %[oa] offset:29696\n"
      "s_waitcnt lgkmcnt(3)\n"
      "v_mfma_f32_32x32x16_bf16 v[40:55], v[218:221], v[80:83], v[40:55]\n"
      "ds_read_b128 v[80:83], %[oa] offset:30720\n"
      "s_waitcnt lgkmcnt(3)\n"
      "v_mfma_f32_32x32x16_bf16 v[40:55], v[222:225], v[84:87], v[40:55]\n"
      "ds_read_b128 v[84:87], %[oa] offset:31744\n"
      "s_waitcnt lgkmcnt(3)\n"
      "v_mfma_f32_32x32x16_bf16 v[40:55], v[226:229], v[88:91], v[40:55]\n"
      "ds_read_b64 v[88:89], %[ob] offset:0\n"
      "ds_read_b64 v[90:91], %[ob] offset:256\n"
      "s_waitcnt lgkmcnt(4)\n"
      "v_mfma_f32_32x32x16_bf16 v[40:55], v[230:233], v[92:95], v[40:55]\n"
      "ds_read_b64 v[92:93], %[ob] offset:1024\n"
      "ds_read_b64 v[94:95], %[ob] offset:1280\n"
      "s_waitcnt lgkmcnt(5)\n"
      "v_mfma_f32_32x32x16_bf16 v[40:55], v[234:237], v[80:83], v[40:55]\n"
      "ds_read_b64 v[80:81], %[ob] offset:2048\n"
      "ds_read_b64 v[82:83], %[ob] offset:2304\n"
      "s_waitcnt lgkmcnt(6)\n"
      "v_mfma_f32_32x32x16_bf16 v[40:55], v[238:241], v[84:87], v[40:55]\n"
      "s_waitcnt lgkmcnt(0)\n"
      "s_barrier\n"
      "ds_read_b128 v[96:99], %[ps] offset:4096\n"
      "ds_read_b128 v[100:103], %[ps] offset:5120\n"
      "ds_read_b128 v[104:107], %[ps] offset:6144\n"
      "ds_read_b128 v[108:111], %[ps] offset:7168\n"
      "ds_read_b128 v[112:115], %[ps] offset:8192\n"
      "ds_read_b128 v[116:119], %[ps] offset:9216\n"
      "ds_read_b128 v[120:123], %[ps] offset:10240\n"
      "ds_read_b128 v[124:127], %[ps] offset:11264\n"
      "v_mfma_f32_32x32x16_bf16 v[40:55], v[242:245], v[88:91], v[40:55]\n"
      "v_mfma_f32_32x32x16_bf16 v[40:55], v[246:249], v[92:95], v[40:55]\n"
      "v_mfma_f32_32x32x16_bf16 v[40:55], v[250:253], v[80:83], v[40:55]\n"
      "s_nop 15\n"
      "s_waitcnt lgkmcnt(0)\n"
      "v_pk_add_f32 v[40:41], v[40:41], v[96:97]\n"
      "v_pk_add_f32 v[42:43], v[42:43], v[98:99]\n"
      "v_pk_add_f32 v[44:45], v[44:45], v[100:101]\n"
      "v_pk_add_f32 v[46:47], v[46:47], v[102:103]\n"
      "v_pk_add_f32 v[48:49], v[48:49], v[104:105]\n"
      "v_pk_add_f32 v[50:51], v[50:51], v[106:107]\n"
      "v_pk_add_f32 v[52:53], v[52:53], v[108:109]\n"
      "v_pk_add_f32 v[54:55], v[54:55], v[110:111]\n"
      "v_pk_add_f32 v[40:41], v[40:41], v[112:113]\n"
      "v_pk_add_f32 v[42:43], v[42:43], v[114:115]\n"
      "v_pk_add_f32 v[44:45], v[44:45], v[116:117]\n"
      "v_pk_add_f32 v[46:47], v[46:47], v[118:119]\n"
      "v_pk_add_f32 v[48:49], v[48:49], v[120:121]\n"
      "v_pk_add_f32 v[50:51], v[50:51], v[122:123]\n"
      "v_pk_add_f32 v[52:53], v[52:53], v[124:125]\n"
      "v_pk_add_f32 v[54:55], v[54:55], v[126:127]\n"
      "s_branch 19f\n"
      "13:\n"
      "ds_read_b64 v[80:81], %[ob] offset:3072\n"
      "ds_read_b64 v[82:83], %[ob] offset:3328\n"
      "ds_read_b64 v[84:85], %[ob] offset:4096\n"
      "ds_read_b64 v[86:87], %[ob] offset:4352\n"
      "ds_read_b64 v[88:89], %[ob] offset:5120\n"
      "ds_read_b64 v[90:91], %[ob] offset:5376\n"
      "ds_read_b64 v[92:93], %[ob] offset:6144\n"
      "ds_read_b64 v[94:95], %[ob] offset:6400\n"
      "s_waitcnt lgkmcnt(6)\n"
      "v_mfma_f32_32x32x16_bf16 v[64:79], v[154:157], v[80:83], 0\n"
      "ds_read_b64 v[80:81], %[ob] offset:7168\n"
      "ds_read_b64 v[82:83], %[ob] offset:7424\n"
      "s_waitcnt lgkmcnt(6)\n"
      "v_mfma_f32_32x32x16_bf16 v[64:79], v[158:161], v[84:87], v[64:79]\n"
      "ds_read_b128 v[84:87], %[oa] offset:0\n"
      "s_waitcnt lgkmcnt(5)\n"
      "v_mfma_f32_32x32x16_bf16 v[64:79], v[162:165], v[88:91], v[64:79]\n"
      "ds_read_b128 v[88:91], %[oa] offset:1024\n"
      "s_waitcnt lgkmcnt(4)\n"
      "v_mfma_f32_32x32x16_bf16 v[64:79], v[166:169], v[92:95], v[64:79]\n"
      "ds_read_b128 v[92:95], %[oa] offset:2048\n"
      "s_waitcnt lgkmcnt(3)\n"
      "v_mfma_f32_32x32x16_bf16 v[64:79], v[170:173], v[80:83], v[64:79]\n"
      "ds_read_b128 v[80:83], %[oa] offset:3072\n"
      "s_waitcnt lgkmcnt(3)\n"
      "v_mfma_f32_32x32x16_bf16 v[40:55], v[174:177], v[84:87], 0\n"
      "ds_read_b128 v[84:87], %[oa] offset:4096\n"
      "s_waitcnt lgkmcnt(3)\n"
      "v_mfma_f32_32x32x16_bf16 v[40:55], v[178:181], v[88:91], v[40:55]\n"
      "ds_read_b128 v[88:91], %[oa] offset:5120\n"
      "s_waitcnt lgkmcnt(3)\n"
      "v_mfma_f32_32x32x16_bf16 v[40:55], v[182:185], v[92:95], v[40:55]\n"
      "ds_read_b128 v[92:95], %[oa] offset:6144\n"
      "s_waitcnt lgkmcnt(3)\n"
      "v_mfma_f32_32x32x16_bf16 v[40:55], v[186:189], v[80:83], v[40:55]\n"
      "ds_read_b128 v[80:83], %[oa] offset:7168\n"
      "s_nop 3\n"
      "ds_write_b128 %[ps], v[64:67] offset:8192\n"
      "ds_write_b128 %[ps], v[68:71] offset:9216\n"
      "ds_write_b128 %[ps], v[72:75] offset:10240\n"
      "ds_write_b128 %[ps], v[76:79] offset:11264\n"
      "s_waitcnt lgkmcnt(7)\n"
      "v_mfma_f32_32x32x16_bf16 v[40:55], v[190:193], v[84:87], v[40:55]\n"
      "ds_read_b128 v[84:87], %[oa] offset:8192\n"
      "s_waitcnt lgkmcnt(7)\n"
      "v_mfma_f32_32x32x16_bf16 v[40:55], v[194:197], v[88:91], v[40:55]\n"
      "ds_read_b128 v[88:91], %[oa] offset:9216\n"
      "s_waitcnt lgkmcnt(7)\n"
      "v_mfma_f32_32x32x16_bf16 v[40:55], v[198:201], v[92:95], v[40:55]\n"
      "ds_read_b128 v[92:95], %[oa] offset:10240\n"
      "s_waitcnt lgkmcnt(7)\n"
      "v_mfma_f32_32x32x16_bf16 v[40:55], v[202:205], v[80:83], v[40:55]\n"
      "ds_read_b128 v[80:83], %[oa] offset:11264\n"
      "s_waitcnt lgkmcnt(3)\n"
      "v_mfma_f32_32x32x16_bf16 v[40:55], v[206:209], v[84:87], v[40:55]\n"
      "ds_read_b128 v[84:87], %[oa] offset:12288\n"
      "s_waitcnt lgkmcnt(3)\n"
      "v_mfma_f32_32x32x16_bf16 v[40:55], v[210:213], v[88:91], v[40:55]\n"
      "ds_read_b128 v[88:91], %[oa] offset:13312\n"
      "s_waitcnt lgkmcnt(3)\n"
      "v_mfma_f32_32x32x16_bf16 v[40:55], v[214:217], v[92:95], v[40:55]\n"
      "ds_read_b128 v[92:95], %[oa] offset:14336\n"
      "s_waitcnt lgkmcnt(3)\n"
      "v_mfma_f32_32x32x16_bf16 v[40:55], v[218:221], v[80:83], v[40:55]\n"
      "ds_read_b128 v[80:83], %[oa] offset:15360\n"
      "s_waitcnt lgkmcnt(3)\n"
      "v_mfma_f32_32x32x16_bf16 v[40:55], v[222:225], v[84:87], v[40:55]\n"
      "ds_read_b128 v[84:87], %[oa] offset:16384\n"
      "s_waitcnt lgkmcnt(3)\n"
      "v_mfma_f32_32x32x16_bf16 v[40:55], v[226:229], v[88:91], v[40:55]\n"
      "ds_read_b128 v[88:91], %[oa] offset:17408\n"
      "s_waitcnt lgkmcnt(3)\n"
      "v_mfma_f32_32x32x16_bf16 v[40:55], v[230:233], v[92:95], v[40:55]\n"
      "ds_read_b128 v[92:95], %[oa] offset:18432\n"
      "s_waitcnt lgkmcnt(3)\n"
      "v_mfma_f32_32x32x16_bf16 v[40:55], v[234:237], v[80:83], v[40:55]\n"
      "ds_read_b128 v[80:83], %[oa] offset:19456\n"
      "s_waitcnt lgkmcnt(3)\n"
      "v_mfma_f32_32x32x16_bf16 v[40:55], v[238:241], v[84:87], v[40:55]\n"
      "s_waitcnt lgkmcnt(0)\n"
      "s_barrier\n"
      "ds_read_b128 v[96:99], %[ps] offset:12288\n"
      "ds_read_b128 v[100:103], %[ps] offset:13312\n"
      "ds_read_b128 v[104:107], %[ps] offset:14336\n"
      "ds_read_b128 v[108:111], %[ps] offset:15360\n"
      "v_mfma_f32_32x32x16_bf16 v[40:55], v[242:245], v[88:91], v[40:55]\n"
      "v_mfma_f32_32x32x16_bf16 v[40:55], v[246:249], v[92:95], v[40:55]\n"
      "v_mfma_f32_32x32x16_bf16 v[40:55], v[250:253], v[80:83], v[40:55]\n"
      "s_nop 15\n"
      "s_waitcnt lgkmcnt(0)\n"
      "v_pk_add_f32 v[40:41], v[40:41], v[96:97]\n"
      "v_pk_add_f32 v[42:43], v[42:43], v[98:99]\n"
      "v_pk_add_f32 v[44:45], v[44:45], v[100:101]\n"
      "v_pk_add_f32 v[46:47], v[46:47], v[102:103]\n"
      "v_pk_add_f32 v[48:49], v[48:49], v[104:105]\n"
      "v_pk_add_f32 v[50:51], v[50:51], v[106:107]\n"
      "v_pk_add_f32 v[52:53], v[52:53], v[108:109]\n"
      "v_pk_add_f32 v[54:55], v[54:55], v[110:111]\n"
      "s_branch 19f\n"
      "14:\n"
      "s_setprio 1\n"
      "ds_read_b128 v[80:83], %[oa] offset:20480\n"
      "ds_read_b128 v[84:87], %[oa] offset:21504\n"
      "ds_read_b128 v[88:91], %[oa] offset:22528\n"
      "ds_read_b128 v[92:95], %[oa] offset:23552\n"
      "s_waitcnt lgkmcnt(3)\n"
      "v_mfma_f32_32x32x16_bf16 v[40:55], v[154:157], v[80:83], 0\n"
      "ds_read_b128 v[80:83], %[oa] offset:24576\n"
      "s_waitcnt lgkmcnt(3)\n"
      "v_mfma_f32_32x32x16_bf16 v[40:55], v[158:161], v[84:87], v[40:55]\n"
      "ds_read_b128 v[84:87], %[oa] offset:25600\n"
      "s_waitcnt lgkmcnt(3)\n"
      "v_mfma_f32_32x32x16_bf16 v[40:55], v[162:165], v[88:91], v[40:55]\n"
      "ds_read_b128 v[88:91], %[oa] offset:26624\n"
      "s_waitcnt lgkmcnt(3)\n"
      "v_mfma_f32_32x32x16_bf16 v[40:55], v[166:169], v[92:95], v[40:55]\n"
      "ds_read_b128 v[92:95], %[oa] offset:27648\n"
      "s_waitcnt lgkmcnt(3)\n"
      "v_mfma_f32_32x32x16_bf16 v[40:55], v[170:173], v[80:83], v[40:55]\n"
      "ds_read_b128 v[80:83], %[oa] offset:28672\n"
      "s_waitcnt lgkmcnt(3)\n"
      "v_mfma_f32_32x32x16_bf16 v[40:55], v[174:177], v[84:87], v[40:55]\n"
      "ds_read_b128 v[84:87], %[oa] offset:29696\n"
      "s_waitcnt lgkmcnt(3)\n"
      "v_mfma_f32_32x32x16_bf16 v[40:55], v[178:181], v[88:91], v[40:55]\n"
      "ds_read_b128 v[88:91], %[oa] offset:30720\n"
      "s_waitcnt lgkmcnt(3)\n"
      "v_mfma_f32_32x32x16_bf16 v[40:55], v[182:185], v[92:95], v[40:55]\n"
      "ds_read_b128 v[92:95], %[oa] offset:31744\n"
      "s_waitcnt lgkmcnt(3)\n"
      "v_mfma_f32_32x32x16_bf16 v[40:55], v[186:189], v[80:83], v[40:55]\n"
      "ds_read_b64 v[80:81], %[ob] offset:0\n"
      "ds_read_b64 v[82:83], %[ob] offset:256\n"
      "s_waitcnt lgkmcnt(4)\n"
      "v_mfma_f32_32x32x16_bf16 v[40:55], v[190:193], v[84:87], v[40:55]\n"
      "ds_read_b64 v[84:85], %[ob] offset:1024\n"
      "ds_read_b64 v[86:87], %[ob] offset:1280\n"
      "s_waitcnt lgkmcnt(5)\n"
      "v_mfma_f32_32x32x16_bf16 v[40:55], v[194:197], v[88:91], v[40:55]\n"
      "ds_read_b64 v[88:89], %[ob] offset:2048\n"
      "ds_read_b64 v[90:91], %[ob] offset:2304\n"
      "s_waitcnt lgkmcnt(6)\n"
      "v_mfma_f32_32x32x16_bf16 v[40:55], v[198:201], v[92:95], v[40:55]\n"
      "ds_read_b64 v[92:93], %[ob] offset:3072\n"
      "ds_read_b64 v[94:95], %[ob] offset:3328\n"
      "s_waitcnt lgkmcnt(6)\n"
      "v_mfma_f32_32x32x16_bf16 v[40:55], v[202:205], v[80:83], v[40:55]\n"
      "ds_read_b64 v[80:81], %[ob] offset:4096\n"
      "ds_read_b64 v[82:83], %[ob] offset:4352\n"
      "s_waitcnt lgkmcnt(6)\n"
      "v_mfma_f32_32x32x16_bf16 v[40:55], v[206:209], v[84:87], v[40:55]\n"
      "ds_read_b64 v[84:85], %[ob] offset:5120\n"
      "ds_read_b64 v[86:87], %[ob] offset:5376\n"
      "s_waitcnt lgkmcnt(6)\n"
      "v_mfma_f32_32x32x16_bf16 v[40:55], v[210:213], v[88:91], v[40:55]\n"
      "ds_read_b64 v[88:89], %[ob] offset:6144\n"
      "ds_read_b64 v[90:91], %[ob] offset:6400\n"
      "s_waitcnt lgkmcnt(6)\n"
      "v_mfma_f32_32x32x16_bf16 v[40:55], v[214:217], v[92:95], v[40:55]\n"
      "ds_read_b64 v[92:93], %[ob] offset:7168\n"
      "ds_read_b64 v[94:95], %[ob] offset:7424\n"
      "s_waitcnt lgkmcnt(6)\n"
      "v_mfma_f32_32x32x16_bf16 v[40:55], v[218:221], v[80:83], v[40:55]\n"
      "ds_read_b128 v[80:83], %[oa] offset:0\n"
      "s_waitcnt lgkmcnt(5)\n"
      "v_mfma_f32_32x32x16_bf16 v[40:55], v[222:225], v[84:87], v[40:55]\n"
      "ds_read_b128 v[84:87], %[oa] offset:1024\n"
      "s_waitcnt lgkmcnt(4)\n"
      "v_mfma_f32_32x32x16_bf16 v[40:55], v[226:229], v[88:91], v[40:55]\n"
      "ds_read_b128 v[88:91], %[oa] offset:2048\n"
      "s_waitcnt lgkmcnt(3)\n"
      "v_mfma_f32_32x32x16_bf16 v[40:55], v[230:233], v[92:95], v[40:55]\n"
      "ds_read_b128 v[92:95], %[oa] offset:3072\n"
      "s_waitcnt lgkmcnt(3)\n"
      "v_mfma_f32_32x32x16_bf16 v[64:79], v[234:237], v[80:83], 0\n"
      "ds_read_b128 v[80:83], %[oa] offset:4096\n"
      "s_waitcnt lgkmcnt(3)\n"
      "v_mfma_f32_32x32x16_bf16 v[64:79], v[238:241], v[84:87], v[64:79]\n"
      "s_waitcnt lgkmcnt(2)\n"
      "v_mfma_f32_32x32x16_bf16 v[64:79], v[242:245], v[88:91], v[64:79]\n"
      "s_waitcnt lgkmcnt(1)\n"
      "v_mfma_f32_32x32x16_bf16 v[64:79], v[246:249], v[92:95], v[64:79]\n"
      "s_nop 3\n"
      "ds_write_b128 %[ps], v[40:43] offset:12288\n"
      "ds_write_b128 %[ps], v[44:47] offset:13312\n"
      "ds_write_b128 %[ps], v[48:51] offset:14336\n"
      "ds_write_b128 %[ps], v[52:55] offset:15360\n"
      "s_waitcnt lgkmcnt(4)\n"
      "v_mfma_f32_32x32x16_bf16 v[64:79], v[250:253], v[80:83], v[64:79]\n"
      "s_nop 15\n"
      "ds_write_b128 %[ps], v[64:67] offset:16384\n"
      "ds_write_b128 %[ps], v[68:71] offset:17408\n"
      "ds_write_b128 %[ps], v[72:75] offset:18432\n"
      "ds_write_b128 %[ps], v[76:79] offset:19456\n"
      "s_waitcnt lgkmcnt(0)\n"
      "s_barrier\n"
      "s_branch 19f\n"
      "15:\n"
      "ds_read_b128 v[80:83], %[oa] offset:5120\n"
      "ds_read_b128 v[84:87], %[oa] offset:6144\n"
      "ds_read_b128 v[88:91], %[oa] offset:7168\n"
      "ds_read_b128 v[92:95], %[oa] offset:8192\n"
      "s_waitcnt lgkmcnt(3)\n"
      "v_mfma_f32_32x32x16_bf16 v[40:55], v[154:157], v[80:83], 0\n"
      "ds_read_b128 v[80:83], %[oa] offset:9216\n"
      "s_waitcnt lgkmcnt(3)\n"
      "v_mfma_f32_32x32x16_bf16 v[40:55], v[158:161], v[84:87], v[40:55]\n"
      "ds_read_b128 v[84:87], %[oa] offset:10240\n"
      "s_waitcnt lgkmcnt(3)\n"
      "v_mfma_f32_32x32x16_bf16 v[40:55], v[162:165], v[88:91], v[40:55]\n"
      "ds_read_b128 v[88:91], %[oa] offset:11264\n"
      "s_waitcnt lgkmcnt(3)\n"
      "v_mfma_f32_32x32x16_bf16 v[40:55], v[166:169], v[92:95], v[40:55]\n"
      "ds_read_b128 v[92:95], %[oa] offset:12288\n"
      "s_waitcnt lgkmcnt(3)\n"
      "v_mfma_f32_32x32x16_bf16 v[40:55], v[170:173], v[80:83], v[40:55]\n"
      "ds_read_b128 v[80:83], %[oa] offset:13312\n"
      "s_waitcnt lgkmcnt(3)\n"
      "v_mfma_f32_32x32x16_bf16 v[40:55], v[174:177], v[84:87], v[40:55]\n"
      "ds_read_b128 v[84:87], %[oa] offset:14336\n"
      "s_waitcnt lgkmcnt(3)\n"
      "v_mfma_f32_32x32x16_bf16 v[40:55], v[178:181], v[88:91], v[40:55]\n"
      "ds_read_b128 v[88:91], %[oa] offset:15360\n"
      "s_waitcnt lgkmcnt(3)\n"
      "v_mfma_f32_32x32x16_bf16 v[40:55], v[182:185], v[92:95], v[40:55]\n"
      "ds_read_b128 v[92:95], %[oa] offset:16384\n"
      "s_waitcnt lgkmcnt(3)\n"
      "v_mfma_f32_32x32x16_bf16 v[40:55], v[186:189], v[80:83], v[40:55]\n"
      "ds_read_b128 v[80:83], %[oa] offset:17408\n"
      "s_waitcnt lgkmcnt(3)\n"
      "v_mfma_f32_32x32x16_bf16 v[40:55], v[190:193], v[84:87], v[40:55]\n"
      "ds_read_b128 v[84:87], %[oa] offset:18432\n"
      "s_waitcnt lgkmcnt(3)\n"
      "v_mfma_f32_32x32x16_bf16 v[40:55], v[194:197], v[88:91], v[40:55]\n"
      "ds_read_b128 v[88:91], %[oa] offset:19456\n"
      "s_waitcnt lgkmcnt(3)\n"
      "v_mfma_f32_32x32x16_bf16 v[40:55], v[198:201], v[92:95], v[40:55]\n"
      "ds_read_b128 v[92:95], %[oa] offset:20480\n"
      "s_waitcnt lgkmcnt(3)\n"
      "v_mfma_f32_32x32x16_bf16 v[40:55], v[202:205], v[80:83], v[40:55]\n"
      "ds_read_b128 v[80:83], %[oa] offset:21504\n"
      "s_waitcnt lgkmcnt(3)\n"
      "v_mfma_f32_32x32x16_bf16 v[40:55], v[206:209], v[84:87], v[40:55]\n"
      "ds_read_b128 v[84:87], %[oa] offset:22528\n"
      "s_waitcnt lgkmcnt(3)\n"
      "v_mfma_f32_32x32x16_bf16 v[40:55], v[210:213], v[88:91], v[40:55]\n"
      "ds_read_b128 v[88:91], %[oa] offset:23552\n"
      "s_waitcnt lgkmcnt(3)\n"
      "v_mfma_f32_32x32x16_bf16 v[40:55], v[214:217], v[92:95], v[40:55]\n"
      "ds_read_b128 v[92:95], %[oa] offset:24576\n"
      "s_waitcnt lgkmcnt(3)\n"
      "v_mfma_f32_32x32x16_bf16 v[40:55], v[218:221], v[80:83], v[40:55]\n"
      "ds_read_b128 v[80:83], %[oa] offset:25600\n"
      "s_waitcnt lgkmcnt(3)\n"
      "v_mfma_f32_32x32x16_bf16 v[40:55], v[222:225], v[84:87], v[40:55]\n"
      "ds_read_b128 v[84:87], %[oa] offset:26624\n"
      "s_waitcnt lgkmcnt(3)\n"
      "v_mfma_f32_32x32x16_bf16 v[40:55], v[226:229], v[88:91], v[40:55]\n"
      "ds_read_b128 v[88:91], %[oa] offset:27648\n"
      "s_waitcnt lgkmcnt(3)\n"
      "v_mfma_f32_32x32x16_bf16 v[40:55], v[230:233], v[92:95], v[40:55]\n"
      "ds_read_b128 v[92:95], %[oa] offset:28672\n"
      "s_waitcnt lgkmcnt(3)\n"
      "v_mfma_f32_32x32x16_bf16 v[40:55], v[234:237], v[80:83], v[40:55]\n"
      "ds_read_b128 v[80:83], %[oa] offset:29696\n"
      "s_waitcnt lgkmcnt(3)\n"
      "v_mfma_f32_32x32x16_bf16 v[40:55], v[238:241], v[84:87], v[40:55]\n"
      "s_waitcnt lgkmcnt(0)\n"
      "s_barrier\n"
      "ds_read_b128 v[96:99], %[ps] offset:16384\n"
      "ds_read_b128 v[100:103], %[ps] offset:17408\n"
      "ds_read_b128 v[104:107], %[ps] offset:18432\n"
      "ds_read_b128 v[108:111], %[ps] offset:19456\n"
      "ds_read_b128 v[112:115], %[ps] offset:20480\n"
      "ds_read_b128 v[116:119], %[ps] offset:21504\n"
      "ds_read_b128 v[120:123], %[ps] offset:22528\n"
      "ds_read_b128 v[124:127], %[ps] offset:23552\n"
      "v_mfma_f32_32x32x16_bf16 v[40:55], v[242:245], v[88:91], v[40:55]\n"
      "v_mfma_f32_32x32x16_bf16 v[40:55], v[246:249], v[92:95], v[40:55]\n"
      "v_mfma_f32_32x32x16_bf16 v[40:55], v[250:253], v[80:83], v[40:55]\n"
      "s_nop 15\n"
      "s_waitcnt lgkmcnt(0)\n"
      "v_pk_add_f32 v[40:41], v[40:41], v[96:97]\n"
      "v_pk_add_f32 v[42:43], v[42:43], v[98:99]\n"
      "v_pk_add_f32 v[44:45], v[44:45], v[100:101]\n"
      "v_pk_add_f32 v[46:47], v[46:47], v[102:103]\n"
      "v_pk_add_f32 v[48:49], v[48:49], v[104:105]\n"
      "v_pk_add_f32 v[50:51], v[50:51], v[106:107]\n"
      "v_pk_add_f32 v[52:53], v[52:53], v[108:109]\n"
      "v_pk_add_f32 v[54:55], v[54:55], v[110:111]\n"
      "v_pk_add_f32 v[40:41], v[40:41], v[112:113]\n"
      "v_pk_add_f32 v[42:43], v[42:43], v[114:115]\n"
      "v_pk_add_f32 v[44:45], v[44:45], v[116:117]\n"
      "v_pk_add_f32 v[46:47], v[46:47], v[118:119]\n"
      "v_pk_add_f32 v[48:49], v[48:49], v[120:121]\n"
      "v_pk_add_f32 v[50:51], v[50:51], v[122:123]\n"
      "v_pk_add_f32 v[52:53], v[52:53], v[124:125]\n"
      "v_pk_add_f32 v[54:55], v[54:55], v[126:127]\n"
      "s_branch 19f\n"
      "16:\n"
      "s_setprio 1\n"
      "ds_read_b128 v[80:83], %[oa] offset:30720\n"
      "ds_read_b128 v[84:87], %[oa] offset:31744\n"
      "ds_read_b64 v[88:89], %[ob] offset:0\n"
      "ds_read_b64 v[90:91], %[ob] offset:256\n"
      "ds_read_b64 v[92:93], %[ob] offset:1024\n"
      "ds_read_b64 v[94:95], %[ob] offset:1280\n"
      "s_waitcnt lgkmcnt(5)\n"
      "v_mfma_f32_32x32x16_bf16 v[40:55], v[154:157], v[80:83], 0\n"
      "ds_read_b64 v[80:81], %[ob] offset:2048\n"
      "ds_read_b64 v[82:83], %[ob] offset:2304\n"
      "s_waitcnt lgkmcnt(6)\n"
      "v_mfma_f32_32x32x16_bf16 v[40:55], v[158:161], v[84:87], v[40:55]\n"
      "ds_read_b64 v[84:85], %[ob] offset:3072\n"
      "ds_read_b64 v[86:87], %[ob] offset:3328\n"
      "s_waitcnt lgkmcnt(6)\n"
      "v_mfma_f32_32x32x16_bf16 v[40:55], v[162:165], v[88:91], v[40:55]\n"
      "ds_read_b64 v[88:89], %[ob] offset:4096\n"
      "ds_read_b64 v[90:91], %[ob] offset:4352\n"
      "s_waitcnt lgkmcnt(6)\n"
      "v_mfma_f32_32x32x16_bf16 v[40:55], v[166:169], v[92:95], v[40:55]\n"
      "ds_read_b64 v[92:93], %[ob] offset:5120\n"
      "ds_read_b64 v[94:95], %[ob] offset:5376\n"
      "s_waitcnt lgkmcnt(6)\n"
      "v_mfma_f32_32x32x16_bf16 v[40:55], v[170:173], v[80:83], v[40:55]\n"
      "ds_read_b64 v[80:81], %[ob] offset:6144\n"
      "ds_read_b64 v[82:83], %[ob] offset:6400\n"
      "s_waitcnt lgkmcnt(6)\n"
      "v_mfma_f32_32x32x16_bf16 v[40:55], v[174:177], v[84:87], v[40:55]\n"
      "ds_read_b64 v[84:85], %[ob] offset:7168\n"
      "ds_read_b64 v[86:87], %[ob] offset:7424\n"
      "s_waitcnt lgkmcnt(6)\n"
      "v_mfma_f32_32x32x16_bf16 v[40:55], v[178:181], v[88:91], v[40:55]\n"
      "ds_read_b128 v[88:91], %[oa] offset:0\n"
      "s_waitcnt lgkmcnt(5)\n"
      "v_mfma_f32_32x32x16_bf16 v[40:55], v[182:185], v[92:95], v[40:55]\n"
      "ds_read_b128 v[92:95], %[oa] offset:1024\n"
      "s_waitcnt lgkmcnt(4)\n"
      "v_mfma_f32_32x32x16_bf16 v[40:55], v[186:189], v[80:83], v[40:55]\n"
      "ds_read_b128 v[80:83], %[oa] offset:2048\n"
      "s_waitcnt lgkmcnt(3)\n"
      "v_mfma_f32_32x32x16_bf16 v[40:55], v[190:193], v[84:87], v[40:55]\n"
      "ds_read_b128 v[84:87], %[oa] offset:3072\n"
      "s_waitcnt lgkmcnt(3)\n"
      "v_mfma_f32_32x32x16_bf16 v[64:79], v[194:197], v[88:91], 0\n"
      "ds_read_b128 v[88:91], %[oa] offset:4096\n"
      "s_waitcnt lgkmcnt(3)\n"
      "v_mfma_f32_32x32x16_bf16 v[64:79], v[198:201], v[92:95], v[64:79]\n"
      "ds_read_b128 v[92:95], %[oa] offset:5120\n"
      "s_waitcnt lgkmcnt(3)\n"
      "v_mfma_f32_32x32x16_bf16 v[64:79], v[202:205], v[80:83], v[64:79]\n"
      "ds_read_b128 v[80:83], %[oa] offset:6144\n"
      "s_waitcnt lgkmcnt(3)\n"
      "v_mfma_f32_32x32x16_bf16 v[64:79], v[206:209], v[84:87], v[64:79]\n"
      "ds_read_b128 v[84:87], %[oa] offset:7168\n"
      "s_nop 3\n"
      "ds_write_b128 %[ps], v[40:43] offset:20480\n"
      "ds_write_b128 %[ps], v[44:47] offset:21504\n"
      "ds_write_b128 %[ps], v[48:51] offset:22528\n"
      "ds_write_b128 %[ps], v[52:55] offset:23552\n"
      "s_waitcnt lgkmcnt(7)\n"
      "v_mfma_f32_32x32x16_bf16 v[64:79], v[210:213], v[88:91], v[64:79]\n"
      "ds_read_b128 v[88:91], %[oa] offset:8192\n"
      "s_waitcnt lgkmcnt(7)\n"
      "v_mfma_f32_32x32x16_bf16 v[64:79], v[214:217], v[92:95], v[64:79]\n"
      "ds_read_b128 v[92:95], %[oa] offset:9216\n"
      "s_waitcnt lgkmcnt(7)\n"
      "v_mfma_f32_32x32x16_bf16 v[64:79], v[218:221], v[80:83], v[64:79]\n"
      "ds_read_b128 v[80:83], %[oa] offset:10240\n"
      "s_waitcnt lgkmcnt(7)\n"
      "v_mfma_f32_32x32x16_bf16 v[64:79], v[222:225], v[84:87], v[64:79]\n"
      "ds_read_b128 v[84:87], %[oa] offset:11264\n"
      "s_waitcnt lgkmcnt(3)\n"
      "v_mfma_f32_32x32x16_bf16 v[64:79], v[226:229], v[88:91], v[64:79]\n"
      "ds_read_b128 v[88:91], %[oa] offset:12288\n"
      "s_waitcnt lgkmcnt(3)\n"
      "v_mfma_f32_32x32x16_bf16 v[64:79], v[230:233], v[92:95], v[64:79]\n"
      "ds_read_b128 v[92:95], %[oa] offset:13312\n"
      "s_waitcnt lgkmcnt(3)\n"
      "v_mfma_f32_32x32x16_bf16 v[64:79], v[234:237], v[80:83], v[64:79]\n"
      "ds_read_b128 v[80:83], %[oa] offset:14336\n"
      "s_waitcnt lgkmcnt(3)\n"
      "v_mfma_f32_32x32x16_bf16 v[64:79], v[238:241], v[84:87], v[64:79]\n"
      "s_waitcnt lgkmcnt(2)\n"
      "v_mfma_f32_32x32x16_bf16 v[64:79], v[242:245], v[88:91], v[64:79]\n"
      "s_waitcnt lgkmcnt(1)\n"
      "v_mfma_f32_32x32x16_bf16 v[64:79], v[246:249], v[92:95], v[64:79]\n"
      "s_waitcnt lgkmcnt(0)\n"
      "v_mfma_f32_32x32x16_bf16 v[64:79], v[250:253], v[80:83], v[64:79]\n"
      "s_nop 15\n"
      "ds_write_b128 %[ps], v[64:67] offset:24576\n"
      "ds_write_b128 %[ps], v[68:71] offset:25600\n"
      "ds_write_b128 %[ps], v[72:75] offset:26624\n"
      "ds_write_b128 %[ps], v[76:79] offset:27648\n"
      "s_waitcnt lgkmcnt(0)\n"
      "s_barrier\n"
      "s_branch 19f\n"
      "17:\n"
      "ds_read_b128 v[80:83], %[oa] offset:15360\n"
      "ds_read_b128 v[84:87], %[oa] offset:16384\n"
      "ds_read_b128 v[88:91], %[oa] offset:17408\n"
      "ds_read_b128 v[92:95], %[oa] offset:18432\n"
      "s_waitcnt lgkmcnt(3)\n"
      "v_mfma_f32_32x32x16_bf16 v[40:55], v[154:157], v[80:83], 0\n"
      "ds_read_b128 v[80:83], %[oa] offset:19456\n"
      "s_waitcnt lgkmcnt(3)\n"
      "v_mfma_f32_32x32x16_bf16 v[40:55], v[158:161], v[84:87], v[40:55]\n"
      "ds_read_b128 v[84:87], %[oa] offset:20480\n"
      "s_waitcnt lgkmcnt(3)\n"
      "v_mfma_f32_32x32x16_bf16 v[40:55], v[162:165], v[88:91], v[40:55]\n"
      "ds_read_b128 v[88:91], %[oa] offset:21504\n"
      "s_waitcnt lgkmcnt(3)\n"
      "v_mfma_f32_32x32x16_bf16 v[40:55], v[166:169], v[92:95], v[40:55]\n"
      "ds_read_b128 v[92:95], %[oa] offset:22528\n"
      "s_waitcnt lgkmcnt(3)\n"
      "v_mfma_f32_32x32x16_bf16 v[40:55], v[170:173], v[80:83], v[40:55]\n"
      "ds_read_b128 v[80:83], %[oa] offset:23552\n"
      "s_waitcnt lgkmcnt(3)\n"
      "v_mfma_f32_32x32x16_bf16 v[40:55], v[174:177], v[84:87], v[40:55]\n"
      "ds_read_b128 v[84:87], %[oa] offset:24576\n"
      "s_waitcnt lgkmcnt(3)\n"
      "v_mfma_f32_32x32x16_bf16 v[40:55], v[178:181], v[88:91], v[40:55]\n"
      "ds_read_b128 v[88:91], %[oa] offset:25600\n"
      "s_waitcnt lgkmcnt(3)\n"
      "v_mfma_f32_32x32x16_bf16 v[40:55], v[182:185], v[92:95], v[40:55]\n"
      "ds_read_b128 v[92:95], %[oa] offset:26624\n"
      "s_waitcnt lgkmcnt(3)\n"
      "v_mfma_f32_32x32x16_bf16 v[40:55], v[186:189], v[80:83], v[40:55]\n"
      "ds_read_b128 v[80:83], %[oa] offset:27648\n"
      "s_waitcnt lgkmcnt(3)\n"
      "v_mfma_f32_32x32x16_bf16 v[40:55], v[190:193], v[84:87], v[40:55]\n"
      "ds_read_b128 v[84:87], %[oa] offset:28672\n"
      "s_waitcnt lgkmcnt(3)\n"
      "v_mfma_f32_32x32x16_bf16 v[40:55], v[194:197], v[88:91], v[40:55]\n"
      "ds_read_b128 v[88:91], %[oa] offset:29696\n"
      "s_waitcnt lgkmcnt(3)\n"
      "v_mfma_f32_32x32x16_bf16 v[40:55], v[198:201], v[92:95], v[40:55]\n"
      "ds_read_b128 v[92:95], %[oa] offset:30720\n"
      "s_waitcnt lgkmcnt(3)\n"
      "v_mfma_f32_32x32x16_bf16 v[40:55], v[202:205], v[80:83], v[40:55]\n"
      "ds_read_b128 v[80:83], %[oa] offset:31744\n"
      "s_waitcnt lgkmcnt(3)\n"
      "v_mfma_f32_32x32x16_bf16 v[40:55], v[206:209], v[84:87], v[40:55]\n"
      "ds_read_b64 v[84:85], %[ob] offset:0\n"
      "ds_read_b64 v[86:87], %[ob] offset:256\n"
      "s_waitcnt lgkmcnt(4)\n"
      "v_mfma_f32_32x32x16_bf16 v[40:55], v[210:213], v[88:91], v[40:55]\n"
      "ds_read_b64 v[88:89], %[ob] offset:1024\n"
      "ds_read_b64 v[90:91], %[ob] offset:1280\n"
      "s_waitcnt lgkmcnt(5)\n"
      "v_mfma_f32_32x32x16_bf16 v[40:55], v[214:217], v[92:95], v[40:55]\n"
      "ds_read_b64 v[92:93], %[ob] offset:2048\n"
      "ds_read_b64 v[94:95], %[ob] offset:2304\n"
      "s_waitcnt lgkmcnt(6)\n"
      "v_mfma_f32_32x32x16_bf16 v[40:55], v[218:221], v[80:83], v[40:55]\n"
      "ds_read_b64 v[80:81], %[ob] offset:3072\n"
      "ds_read_b64 v[82:83], %[ob] offset:3328\n"
      "s_waitcnt lgkmcnt(6)\n"
      "v_mfma_f32_32x32x16_bf16 v[40:55], v[222:225], v[84:87], v[40:55]\n"
      "ds_read_b64 v[84:85], %[ob] offset:4096\n"
      "ds_read_b64 v[86:87], %[ob] offset:4352\n"
      "s_waitcnt lgkmcnt(6)\n"
      "v_mfma_f32_32x32x16_bf16 v[40:55], v[226:229], v[88:91], v[40:55]\n"
      "ds_read_b64 v[88:89], %[ob] offset:5120\n"
      "ds_read_b64 v[90:91], %[ob] offset:5376\n"
      "s_waitcnt lgkmcnt(6)\n"
      "v_mfma_f32_32x32x16_bf16 v[40:55], v[230:233], v[92:95], v[40:55]\n"
      "ds_read_b64 v[92:93], %[ob] offset:6144\n"
      "ds_read_b64 v[94:95], %[ob] offset:6400\n"
      "s_waitcnt lgkmcnt(6)\n"
      "v_mfma_f32_32x32x16_bf16 v[40:55], v[234:237], v[80:83], v[40:55]\n"
      "ds_read_b64 v[80:81], %[ob] offset:7168\n"
      "ds_read_b64 v[82:83], %[ob] offset:7424\n"
      "s_waitcnt lgkmcnt(6)\n"
      "v_mfma_f32_32x32x16_bf16 v[40:55], v[238:241], v[84:87], v[40:55]\n"
      "s_waitcnt lgkmcnt(0)\n"
      "s_barrier\n"
      "ds_read_b128 v[96:99], %[ps] offset:24576\n"
      "ds_read_b128 v[100:103], %[ps] offset:25600\n"
      "ds_read_b128 v[104:107], %[ps] offset:26624\n"
      "ds_read_b128 v[108:111], %[ps] offset:27648\n"
      "v_mfma_f32_32x32x16_bf16 v[40:55], v[242:245], v[88:91], v[40:55]\n"
      "v_mfma_f32_32x32x16_bf16 v[40:55], v[246:249], v[92:95], v[40:55]\n"
      "v_mfma_f32_32x32x16_bf16 v[40:55], v[250:253], v[80:83], v[40:55]\n"
      "s_nop 15\n"
      "s_waitcnt lgkmcnt(0)\n"
      "v_pk_add_f32 v[40:41], v[40:41], v[96:97]\n"
      "v_pk_add_f32 v[42:43], v[42:43], v[98:99]\n"
      "v_pk_add_f32 v[44:45], v[44:45], v[100:101]\n"
      "v_pk_add_f32 v[46:47], v[46:47], v[102:103]\n"
      "v_pk_add_f32 v[48:49], v[48:49], v[104:105]\n"
      "v_pk_add_f32 v[50:51], v[50:51], v[106:107]\n"
      "v_pk_add_f32 v[52:53], v[52:53], v[108:109]\n"
      "v_pk_add_f32 v[54:55], v[54:55], v[110:111]\n"
      "19:\n"
      : "=&{v[40:55]}"(c)
      : [w] "s"(w), [oa] "v"(oa), [ob] "v"(ob), [ps] "v"(ps), WR("", ""), [tag] "v"(tag), [po] "v"(po2), [ws] "s"(ws), [om0] "s"(om0),
        [om1] "s"(om1), [m0b] "s"(m0b), [og1] "s"(og1), [ot] "s"(ot), [l16] "v"(l16)
      : "memory", "scc", "vcc", "s44", "v64", "v65", "v66", "v67", "v68", "v69", V10(7), V10(8), V10(9), V10(10), V10(11), "v120",
        "v121", "v122", "v123", "v124", "v125", "v126", "v127");
    if ((0xad >> w) & 1) {
      u32x4 a = pk8(c, 0), b = pk8(c, 8);
#pragma unroll
      for (int i = 0; i < 4; ++i) {
        const auto s = __builtin_amdgcn_permlane16_swap(a[i], b[i], false, false);
        a[i] = s[0]; b[i] = s[1];
      }
      asm volatile("global_store_dwordx4 %0, %1, %2\n\ts_nop 1" ::"v"(odst), "v"(a), "s"(out) : "memory");
      asm volatile("global_store_dwordx4 %0, %1, %2\n\ts_nop 1" ::"v"(odst + 16 * OUTW * 2), "v"(b), "s"(out) : "memory");
    }
  }
}

extern "C" void kernel_launch(void* const* d_in, const int* in_sizes, int n_in, void* d_out, int out_size, void* d_ws,
                              size_t ws_size, hipStream_t stream) {
  const uint16_t* const* in = (const uint16_t* const*)d_in;
  fused_kernel<<<256, 512, 0, stream>>>(in[0], in[1], in[2], in[3], in[4], in[5], in[6], in[7], in[8], in[9], (uint16_t*)d_out,
                                        (char*)d_ws + 0, QTAB);
}
```
